```python
import torch

def _softmax(x, axis=-1):
    return torch.softmax(x, dim=axis)
def _gelu(x):
    return torch.nn.functional.gelu(x, approximate="tanh")
def _silu(x):
    return torch.nn.functional.silu(x)
def _sigmoid(x):
    return torch.sigmoid(x)
def _layernorm(x, axis=-1, eps=1e-5):
    m = x.mean(dim=axis, keepdim=True)
    v = x.var(dim=axis, keepdim=True, unbiased=False)
    return (x - m) / torch.sqrt(v + eps)
def _scatter_add(tgt, idx, src, axis=0):
    return tgt.clone().index_add_(axis, idx, src)
def _segment_sum(src, seg, n):
    out = torch.zeros((n,) + src.shape[1:], dtype=src.dtype)
    return out.index_add_(0, seg, src)
def _pad(x, pw):
    flat = tuple(v for pair in reversed(pw) for v in pair)
    return torch.nn.functional.pad(x, flat)

kernel_name = 'parallel_sum__mha_block_cached__swiglu_ffn__decode'

def setup_inputs():

    _w = (lambda a: (lambda t: t.to(torch.bfloat16) if t.is_floating_point() else t)(torch.as_tensor(a)))
    def _A_setup():
        B, Sq, D = 1, 1, 640
        H, Sk, Dh = 32, 16384, 128
        return {
            "x": _w((lambda *s: torch.randn(*s, dtype=torch.bfloat16) * 0.05)(B, Sq, D)),
            "k_cache": _w((lambda *s: torch.randn(*s, dtype=torch.bfloat16) * 3.2)(B, H, Sk, Dh)),
            "v_cache": _w((lambda *s: torch.randn(*s, dtype=torch.bfloat16) * 1.6)(B, H, Sk, Dh)),
            "wq": _w((lambda *s: torch.randn(*s, dtype=torch.bfloat16) * 3.2)(D, H * Dh) * (D ** -0.5)),
            "wo": _w((lambda *s: torch.randn(*s, dtype=torch.bfloat16) * 1.6)(H * Dh, D) * ((H * Dh) ** -0.5)),
        }
    _A_in = _A_setup()

    def _B_setup():
        B, S, D, F = 1, 1, 640, 1728
        return {
            "x": _w((lambda *s: torch.randn(*s, dtype=torch.bfloat16) * 0.05)(B, S, D)),
            "w_gate": _w((lambda *s: torch.randn(*s, dtype=torch.bfloat16) * 0.05)(D, F)),
            "w_up": _w((lambda *s: torch.randn(*s, dtype=torch.bfloat16) * 0.05)(D, F)),
            "w_down": _w((lambda *s: torch.randn(*s, dtype=torch.bfloat16) * 0.05)(F, D)),
        }
    _B_in = _B_setup()

    return {
        "A_x": _A_in["x"],
        "A_k_cache": _A_in["k_cache"],
        "A_v_cache": _A_in["v_cache"],
        "A_wq": _A_in["wq"],
        "A_wo": _A_in["wo"],
        "B_x": _B_in["x"],
        "B_w_gate": _B_in["w_gate"],
        "B_w_up": _B_in["w_up"],
        "B_w_down": _B_in["w_down"],
    }

def reference(A_x, A_k_cache, A_v_cache, A_wq, A_wo, B_x, B_w_gate, B_w_up, B_w_down):
    def _A_ref(x, k_cache, v_cache, wq, wo):
        B, Sq, D = x.shape
        H, Dh = 32, 128
        q = (lambda x, *d: x.permute(*d))((x @ wq).reshape(B, Sq, H, Dh), 0, 2, 1, 3)
        scores = torch.einsum("bhqd,bhkd->bhqk", q, k_cache) * (Dh ** -0.5)
        p = _softmax(scores, axis=-1)
        o = torch.einsum("bhqk,bhkd->bhqd", p, v_cache)
        return (lambda x, *d: x.permute(*d))(o, 0, 2, 1, 3).reshape(B, Sq, H * Dh) @ wo

    def _B_ref(x, w_gate, w_up, w_down):
        g = _silu(x @ w_gate)
        u = x @ w_up
        return (g * u) @ w_down

    x = A_x
    xn = _layernorm(x, axis=-1)
    return x + _A_ref(xn, A_k_cache, A_v_cache, A_wq, A_wo) + _B_ref(xn, B_w_gate, B_w_up, B_w_down)
```

```cpp
#include <hip/hip_runtime.h>
#include <stdint.h>

namespace {

typedef unsigned u32;
typedef unsigned long long u64;
typedef u32 u32x4 __attribute__((ext_vector_type(4)));
typedef u32 u32x2 __attribute__((ext_vector_type(2)));
typedef float f32x2 __attribute__((ext_vector_type(2)));
typedef float f32x4 __attribute__((ext_vector_type(4)));
typedef __bf16 bf16x2 __attribute__((ext_vector_type(2)));
typedef __bf16 bf16x8 __attribute__((ext_vector_type(8)));
typedef __amdgpu_buffer_rsrc_t rsrc_t;
#define GAS __attribute__((address_space(1)))
#define CAS __attribute__((address_space(4)))
#define SB __builtin_amdgcn_sched_barrier(0)

constexpr int DM = 640, NH = 32, SK = 16384, DH = 128, FF = 1728;
constexpr int NSPLIT = 8, NWG = NH * NSPLIT, NW = 8, NT = NW * 64;
constexpr int KEYS_WG = SK / NSPLIT;
constexpr u32 KV_BYTES = KEYS_WG * DH * 2, ROW = NW * 1024;
constexpr int NB = 4, NBT = KV_BYTES / ROW / NB;
constexpr int QROWS = DM / NSPLIT;
constexpr int NSL = 5;
constexpr int FROWS = FF / NH;
constexpr int FLINES = FF / 64;
constexpr u32 NTILE = NH * NSL, NRED = DM / (4 * NW);
constexpr u32 OOB = 0x7ff00000u;
constexpr int NT_ = 2, SC1 = 16;

constexpr u32 WS_EPOCH = 0;
constexpr u32 WS_TICKET = 2048;
constexpr u32 WS_QP = 4096;
constexpr u32 WS_HG = WS_QP + NWG * 1024;
constexpr u32 REC1 = 1280;
constexpr u32 WS_H1 = WS_HG + FLINES * 8 * 1024;
constexpr u32 WS_H2 = WS_H1 + NWG * REC1;
constexpr u32 WS_END = WS_H2 + NSL * NH * 1024;
constexpr u32 TICKET0 = 0x400;
constexpr u32 SPIN_CAP = 1u << 18;

constexpr float LOG2E = 1.4426950408889634f;
constexpr float SCALE = 0.08838834764831845f;

__device__ __forceinline__ float asf(u32 x) { return __builtin_bit_cast(float, x); }
__device__ __forceinline__ u32 asu(float x) { return __builtin_bit_cast(u32, x); }
__device__ __forceinline__ float lo16(u32 x) { return asf(x << 16); }
__device__ __forceinline__ float hi16(u32 x) { return asf(x & 0xffff0000u); }
__device__ __forceinline__ u32 pk_bf16(float a, float b) { bf16x2 v = {(__bf16)a, (__bf16)b}; return __builtin_bit_cast(u32, v); }
__device__ __forceinline__ float rbf(float x) { return (float)(__bf16)x; }

template <int CTRL> __device__ __forceinline__ u32 dppu(u32 x) { return __builtin_amdgcn_mov_dpp(x, CTRL, 0xf, 0xf, true); }
template <int CTRL> __device__ __forceinline__ float dpp(float x) { return asf(dppu<CTRL>(asu(x))); }
constexpr int XOR1 = 0xB1, XOR2 = 0x4E, HMIR = 0x141, ROR4 = 0x124, ROR8 = 0x128;
constexpr int BC0 = 0x00, BC1 = 0x55, BC2 = 0xAA, BC3 = 0xFF;
__device__ __forceinline__ float row_sum(float x) {
  x += dpp<XOR1>(x); x += dpp<XOR2>(x); x += dpp<ROR4>(x); x += dpp<ROR8>(x); return x;
}
__device__ __forceinline__ float fold16(float a, float b) {
  auto s = __builtin_amdgcn_permlane16_swap(asu(a), asu(b), false, false);
  return asf(s[0]) + asf(s[1]);
}
__device__ __forceinline__ float fold32(float x) {
  auto s = __builtin_amdgcn_permlane32_swap(asu(x), asu(x), false, false);
  return asf(s[0]) + asf(s[1]);
}
__device__ __forceinline__ float xrow_sum(float x) { return fold32(fold16(x, x)); }
__device__ __forceinline__ float xrow_max(float x) {
  auto s = __builtin_amdgcn_permlane16_swap(asu(x), asu(x), false, false);
  x = fmaxf(asf(s[0]), asf(s[1]));
  auto t = __builtin_amdgcn_permlane32_swap(asu(x), asu(x), false, false);
  return fmaxf(asf(t[0]), asf(t[1]));
}
__device__ __forceinline__ float oct_sum(float x) { return xrow_sum(x + dpp<ROR8>(x)); }
__device__ __forceinline__ float oct_max(float x) { return xrow_max(fmaxf(x, dpp<ROR8>(x))); }

__device__ __forceinline__ f32x4 mm(u32x4 a, u32x4 b, f32x4 acc) {
  return __builtin_amdgcn_mfma_f32_16x16x32_bf16(__builtin_bit_cast(bf16x8, a), __builtin_bit_cast(bf16x8, b), acc, 0, 0, 0);
}
__device__ __forceinline__ u32x4 diag_sel(int j) {
  const u32 half = (j & 1) ? 0xffff0000u : 0x0000ffffu;
  return u32x4{(j >> 1) == 0 ? half : 0u, (j >> 1) == 1 ? half : 0u, (j >> 1) == 2 ? half : 0u, (j >> 1) == 3 ? half : 0u};
}
__device__ __forceinline__ u32x4 diag(u32 xx, u32x4 sel) { return u32x4{xx & sel[0], xx & sel[1], xx & sel[2], xx & sel[3]}; }
__device__ __forceinline__ f32x4 vecmat(u32x4 w, u32 xx, u32x4 sel, f32x4 acc) { return mm(w, diag(xx, sel), acc); }
__device__ __forceinline__ u32 acc_pos(u32 e) { return ((e >> 5) * 8 + (e & 7)) * 4 + ((e >> 3) & 3); }

__device__ __forceinline__ rsrc_t mkrsrc(const void* p, u32 bytes) {
  return __builtin_amdgcn_make_buffer_rsrc(const_cast<void*>(p), 0, bytes, 0x00020000);
}
template <int AUX> __device__ __forceinline__ u32x4 bld(rsrc_t r, u32 voff, u32 soff) {
  return __builtin_amdgcn_raw_buffer_load_b128(r, voff, soff, AUX);
}
__device__ __forceinline__ void st_unit(rsrc_t r, u32 off, float a, float b, u32 tag) {
  __builtin_amdgcn_raw_buffer_store_b128(u32x4{asu(a), tag, asu(b), tag}, r, off, 0, SC1);
}
__device__ __forceinline__ u32x4 ld_unit(rsrc_t r, u32 off) { return bld<SC1>(r, off, 0); }
__device__ __forceinline__ u32x2 ld_gran(rsrc_t r, u32 off) { return __builtin_amdgcn_raw_buffer_load_b64(r, off, 0, SC1); }
__device__ __forceinline__ bool fresh(u32x4 u, u32 tag) { return u[1] == tag && u[3] == tag; }

#define POLL_GAP "s_sleep 5\n\t"
#define POLL_TAIL(SLP, L)                                   \
  "s_andn2_b64 %[need], %[need], vcc\n\t"                   \
  "s_cbranch_scc0 2f\n\t"                                   \
  "s_bcnt1_i32_b64 %[n], %[need]\n\t"                       \
  "s_cmp_le_u32 %[n], 8\n\t"                                \
  "s_cbranch_scc1 " L "f\n\t"                               \
  "s_sleep " SLP "\n"                                       \
  L ":\n\t"                                                 \
  "s_mov_b64 exec, %[need]\n\t"
#define POLL_LOOP_END                                       \
  "s_sub_u32 %[cnt], %[cnt], 1\n\t"                         \
  "s_cbranch_scc0 1b\n"                                     \
  "2:\n\t"                                                  \
  "s_nop 1"
#define POLL2_LD(A) "buffer_load_dwordx4 v[" #A ":" #A "+3], %[o], %[rs], 0 offen sc1\n\t"
#define POLL2_PASS(A, L)                                    \
  "s_waitcnt vmcnt(2)\n\t"                                  \
  "v_cmp_eq_u32 vcc, %[tag], v[" #A "+1]\n\t"               \
  "v_cmp_eq_u32 %[t], %[tag], v[" #A "+3]\n\t"              \
  "s_and_b64 vcc, vcc, %[t]\n\t"                            \
  "s_and_b64 vcc, vcc, %[need]\n\t"                         \
  "v_cndmask_b32 %[u0], %[u0], v[" #A "], vcc\n\t"          \
  "v_cndmask_b32 %[u1], %[u1], v[" #A "+2], vcc\n\t"        \
  POLL_TAIL("8", L) POLL2_LD(A) "s_mov_b64 exec, -1\n\t"
__device__ __forceinline__ void poll_units(rsrc_t rs, u32 o, u32 tag, u64 need, u32& u0, u32& u1) {
  u64 t; u32 n, cnt = SPIN_CAP;
  asm volatile(
      "s_mov_b64 exec, %[need]\n\t" POLL2_LD(208) POLL_GAP POLL2_LD(212) POLL_GAP POLL2_LD(216) "s_mov_b64 exec, -1\n"
      "1:\n\t" POLL2_PASS(208, "3") POLL2_PASS(212, "4") POLL2_PASS(216, "5") POLL_LOOP_END
      : [u0] "+v"(u0), [u1] "+v"(u1), [need] "+s"(need), [cnt] "+s"(cnt), [t] "=&s"(t), [n] "=&s"(n)
      : [o] "v"(o), [rs] "s"(rs), [tag] "s"(tag)
      : "vcc", "scc", "memory", "v208", "v209", "v210", "v211", "v212", "v213", "v214", "v215", "v216", "v217", "v218", "v219");
}
#define POLL1_LD(A)                                                              \
  "buffer_load_dwordx4 v[" #A ":" #A "+3], %[oa], %[rs], 0 offen sc1\n\t"        \
  "buffer_load_dwordx4 v[" #A "+4:" #A "+7], %[om], %[rs], 0 offen sc1\n\t"
#define POLL1_PASS(A, L)                                    \
  "s_waitcnt vmcnt(4)\n\t"                                  \
  "v_cmp_eq_u32 vcc, %[tag], v[" #A "+1]\n\t"               \
  "v_cmp_eq_u32 %[t], %[tag], v[" #A "+3]\n\t"              \
  "s_and_b64 %[g], vcc, %[t]\n\t"                           \
  "v_cmp_eq_u32 vcc, %[tag], v[" #A "+5]\n\t"               \
  "v_cmp_eq_u32 %[t], %[tag], v[" #A "+7]\n\t"              \
  "s_and_b64 %[g], %[g], %[need]\n\t"                       \
  "s_and_b64 vcc, vcc, %[t]\n\t"                            \
  "s_and_b64 vcc, vcc, %[g]\n\t"                            \
  "v_cndmask_b32 %[a0], %[a0], v[" #A "], vcc\n\t"          \
  "v_cndmask_b32 %[a1], %[a1], v[" #A "+2], vcc\n\t"        \
  "v_cndmask_b32 %[m0], %[m0], v[" #A "+4], vcc\n\t"        \
  "v_cndmask_b32 %[m1], %[m1], v[" #A "+6], vcc\n\t"        \
  POLL_TAIL("2", L) POLL1_LD(A) "s_mov_b64 exec, -1\n\t"
__device__ __forceinline__ void poll_records(rsrc_t rs, u32 oa, u32 om, u32 tag, u64 need, u32& a0, u32& a1, u32& m0, u32& m1) {
  u64 t, g; u32 n, cnt = SPIN_CAP;
  asm volatile(
      "s_mov_b64 exec, %[need]\n\t" POLL1_LD(208) POLL_GAP POLL1_LD(216) POLL_GAP POLL1_LD(224) "s_mov_b64 exec, -1\n"
      "1:\n\t" POLL1_PASS(208, "3") POLL1_PASS(216, "4") POLL1_PASS(224, "5") POLL_LOOP_END
      : [a0] "+v"(a0), [a1] "+v"(a1), [m0] "+v"(m0), [m1] "+v"(m1), [need] "+s"(need), [cnt] "+s"(cnt), [t] "=&s"(t),
        [g] "=&s"(g), [n] "=&s"(n)
      : [oa] "v"(oa), [om] "v"(om), [rs] "s"(rs), [tag] "s"(tag)
      : "vcc", "scc", "memory", "v208", "v209", "v210", "v211", "v212", "v213", "v214", "v215", "v216", "v217", "v218", "v219",
        "v220", "v221", "v222", "v223", "v224", "v225", "v226", "v227", "v228", "v229", "v230", "v231");
}

struct Attn { float m, l; f32x4 acc; };

__device__ __forceinline__ float quad_trace(f32x4 d) {
  float t = ((dpp<BC0>(d[0]) + dpp<BC1>(d[1])) + dpp<BC2>(d[2])) + dpp<BC3>(d[3]);
  asm("" : "+v"(t));
  return t;
}
__device__ __forceinline__ u32 softmax_step(const f32x4 (&d)[NB], int slot, Attn& a) {
  const float t0 = quad_trace(d[0]), t1 = quad_trace(d[1]), t2 = quad_trace(d[2]), t3 = quad_trace(d[3]);
  const float t = (slot & 2) ? ((slot & 1) ? t3 : t2) : ((slot & 1) ? t1 : t0);
  const float sv = rbf(rbf(t) * SCALE) * LOG2E;
  const float mx = fmaxf(sv, dpp<XOR1>(sv));
  const float mn = fmaxf(a.m, xrow_max(fmaxf(mx, dpp<XOR2>(mx))));
  const float alpha = __builtin_amdgcn_exp2f(a.m - mn), p = __builtin_amdgcn_exp2f(sv - mn);
  a.m = mn;
  a.l = fmaf(a.l, alpha, p);
  a.acc *= alpha;
  return pk_bf16(p, p);
}
__device__ __forceinline__ u32 next_row(u32 o) { return (o + ROW) & (KV_BYTES - 1); }

__global__ void __launch_bounds__(NT, 2)
fused_decode(const u32* __restrict__ x32, const char* __restrict__ kc, const char* __restrict__ vc,
             const char* __restrict__ wq, const char* __restrict__ wo, const char* __restrict__ wg,
             const char* __restrict__ wu, const char* __restrict__ wd, u32* __restrict__ out32, char* ws) {
  __shared__ __attribute__((aligned(16))) float lds[5 * NW * 128 + 64 + 2 * NW + 4];
  u32* xn_s = reinterpret_cast<u32*>(lds);
  float* qp_s = lds + NW * 128;
  float* fp_s = qp_s + NW * 128;
  float* op_s = fp_s + NW * 128;
  float* yp_s = op_s + NW * 128;
  u32* q_s = reinterpret_cast<u32*>(yp_s + NW * 128);
  float* ml_s = yp_s + NW * 128 + 64;
  u32* role_s = reinterpret_cast<u32*>(ml_s + 2 * NW);

  asm volatile("" :: "s"(x32), "s"(kc), "s"(vc), "s"(wq), "s"(wo), "s"(wg), "s"(wu), "s"(wd), "s"(out32), "s"(ws));
  const int tid = threadIdx.x, lane = tid & 63, r = lane >> 4, c = lane & 15;
  const int w = __builtin_amdgcn_readfirstlane(tid >> 6);
  const int b = blockIdx.x, xs = b & 7, j = b >> 3;
  const int h = xs * 4 + (j >> 3), s = j & 7;
  const rsrc_t wsr = mkrsrc(ws, WS_END);
  const f32x4 zero4 = {0.f, 0.f, 0.f, 0.f};

  const int qrow = s * QROWS + 4 * w;
  const bool ffn_on = b < FLINES * 8;
  const int frow = xs * 80 + 8 * (w & 3);
  const size_t kv0 = (size_t)(h * NSPLIT + s) * KV_BYTES;
  const rsrc_t kr = mkrsrc(kc + kv0, KV_BYTES), vr = mkrsrc(vc + kv0, KV_BYTES);
  const u32 vo = lane * 16 + w * 1024;
  const u32 ko = (c >> 2) * 256 + (c & 3) * 64 + r * 16 + w * 1024;
  u32 ro[NB];
  ro[0] = ((u32)(h * NSPLIT + s) * ROW) & (KV_BYTES - 1);
#pragma unroll
  for (int i = 1; i < NB; ++i) ro[i] = next_row(ro[i - 1]);
  auto load_k = [&](u32x4 (&X)[NB], bool advance) {
#pragma unroll
    for (int i = 0; i < NB; ++i) { if (advance) ro[i] = next_row(ro[(i + NB - 1) % NB]); X[i] = bld<NT_>(kr, ko, ro[i]); }
    SB;
  };
  auto load_v = [&](u32x4 (&X)[NB]) {
#pragma unroll
    for (int i = 0; i < NB; ++i) X[i] = bld<NT_>(vr, vo, ro[i]);
    SB;
  };
  u32x4 K[NB], V[NB], KB[NB], VB[NB], KC[NB], VC[NB];
  const rsrc_t xrs = mkrsrc(x32, DM * 2);
  const u32x4 xa = bld<0>(xrs, lane * 16, 0), xb = bld<0>(xrs, lane * 16 + 1024, 0);
  u32 tag = *(const CAS u32*)(ws + WS_EPOCH) + 1u;
  u32x4 WQ[3];
  {
    const rsrc_t qr = mkrsrc(wq, DM * NH * DH * 2);
    const u32 o = (u32)((qrow + r) * (NH * DH) + h * DH + 8 * c) * 2;
    WQ[0] = bld<NT_>(qr, o, 0);
    WQ[1] = bld<NT_>(qr, o + 32 * NH * DH * 2, 0);
    WQ[2] = bld<NT_>(qr, w < 4 ? o + 64 * NH * DH * 2 : OOB, 0);
    SB;
  }
  load_k(K, false);

  {
    float xv[16], sum = 0.f;
#pragma unroll
    for (int i = 0; i < 4; ++i) { xv[2 * i] = lo16(xa[i]); xv[2 * i + 1] = hi16(xa[i]); xv[8 + 2 * i] = lo16(xb[i]); xv[9 + 2 * i] = hi16(xb[i]); }
#pragma unroll
    for (int i = 0; i < 16; ++i) sum += xv[i];
    const float mean = xrow_sum(row_sum(sum)) * (1.f / DM);
    float sq = 0.f, sq2 = 0.f;
#pragma unroll
    for (int i = 0; i < 8; ++i) { const float d = xv[i] - mean, e = xv[8 + i] - mean; sq = fmaf(d, d, sq); sq2 = fmaf(e, e, sq2); }
    const float var = xrow_sum(row_sum(lane < 16 ? sq + sq2 : sq)) * (1.f / DM);
    const float mb = rbf(mean);
    const float inv = __builtin_amdgcn_rcpf(rbf(__builtin_amdgcn_sqrtf(rbf(rbf(var) + 1e-5f))));
    u32 n[16];
#pragma unroll
    for (int i = 0; i < 16; ++i) { const float t = rbf(xv[i] - mb) * inv; n[i] = pk_bf16(t, t); }
    *reinterpret_cast<u32x4*>(xn_s + 8 * lane) = u32x4{n[0], n[1], n[2], n[3]};
    *reinterpret_cast<u32x4*>(xn_s + 8 * lane + 4) = u32x4{n[4], n[5], n[6], n[7]};
    *reinterpret_cast<u32x4*>(xn_s + 512 + 8 * lane) = u32x4{n[8], n[9], n[10], n[11]};
    *reinterpret_cast<u32x4*>(xn_s + 512 + 8 * lane + 4) = u32x4{n[12], n[13], n[14], n[15]};
  }
  SB;
  load_v(V);
  load_k(KB, true);

  const u32x4 sel16 = diag_sel(c);
  {
    f32x4 a = zero4;
#pragma unroll
    for (int i = 0; i < 3; ++i) a = vecmat(WQ[i], xn_s[qrow + 32 * i + r], sel16, a);
    if (c < 8) *reinterpret_cast<f32x4*>(qp_s + w * 128 + (r * 8 + c) * 4) = a;
  }
  asm volatile("" : "+s"(tag));
  __syncthreads();
  if (w == 0) {
    const float* p = qp_s + acc_pos(2 * lane);
    float t0 = 0.f, t1 = 0.f;
#pragma unroll
    for (int i = 0; i < NW; ++i) { t0 += p[i * 128]; t1 += p[i * 128 + 4]; }
    st_unit(wsr, WS_QP + (h * NSPLIT + s) * 1024 + lane * 16, t0, t1, tag);
    if (b == 0 && lane == 0)
      __hip_atomic_store((GAS u32*)(ws + WS_TICKET), (tag << 12) + TICKET0, __ATOMIC_RELAXED, __HIP_MEMORY_SCOPE_AGENT);
  }
  SB;

  u32x4 WF[3];
  {
    const rsrc_t fr = mkrsrc(w < 4 ? wg : wu, ffn_on ? DM * FF * 2 : 0);
    const u32 o = (u32)((frow + (lane >> 3)) * FF + j * 64 + 8 * (lane & 7)) * 2;
    WF[0] = bld<NT_>(fr, o, 0);
    WF[1] = bld<NT_>(fr, o + 32 * FF * 2, 0);
    WF[2] = bld<NT_>(fr, (w & 3) < 2 ? o + 64 * FF * 2 : OOB, 0);
    SB;
  }
  load_v(VB);
  load_k(KC, true);
  const u32 qoff = WS_QP + h * NSPLIT * 1024 + lane * 16;
  u32x4 qu[NSPLIT];
  if (w == 0) {
#pragma unroll
    for (int i = 0; i < NSPLIT; ++i) qu[i] = ld_unit(wsr, qoff + i * 1024);
  }
  SB;
  load_v(VC);
  if (w == 0) {
    auto all_fresh = [&]() {
      bool ok = true;
#pragma unroll
      for (int i = 0; i < NSPLIT; ++i) ok = ok && fresh(qu[i], tag);
      return __all(ok);
    };
    if (!all_fresh()) {
#pragma nounroll
      for (u32 spin = 0; spin < SPIN_CAP; ++spin) {
        asm volatile("" ::: "memory");
#pragma unroll
        for (int i = 0; i < NSPLIT; ++i) qu[i] = ld_unit(wsr, qoff + i * 1024);
        if (all_fresh()) break;
      }
    }
    float t0 = 0.f, t1 = 0.f;
#pragma unroll
    for (int i = 0; i < NSPLIT; ++i) { t0 += asf(qu[i][0]); t1 += asf(qu[i][2]); }
    q_s[lane] = pk_bf16(t0, t1);
  }
  {
    const u32x4 sel8 = diag_sel(lane & 7);
    f32x4 f = zero4;
#pragma unroll
    for (int i = 0; i < 3; ++i) f = vecmat(WF[i], xn_s[frow + 32 * i + (lane >> 3)], sel8, f);
    if ((c >> 3) == (r >> 1)) *reinterpret_cast<f32x4*>(fp_s + w * 128 + (r * 8 + (c & 7)) * 4) = f;
  }
  __syncthreads();
  const u32x4 q = *reinterpret_cast<const u32x4*>(q_s + 16 * (c & 3) + 4 * r);
  if (w == NW - 1 && ffn_on) {
    const float* p = fp_s + (lane >> 5) * 512 + acc_pos(2 * (lane & 31));
    float g0 = 0.f, g1 = 0.f;
#pragma unroll
    for (int i = 0; i < 8; ++i) { g0 += p[i * 64]; g1 += p[i * 64 + 4]; }
    st_unit(wsr, WS_HG + b * 1024 + lane * 16, g0, g1, tag);
  }

  Attn a;
  a.m = -INFINITY; a.l = 0.f; a.acc = zero4;
  f32x4 d[NB];
  auto ring_step = [&]() {
#pragma unroll
    for (int i = 0; i < NB; ++i) {
      d[i] = mm(K[i], q, zero4); SB;
      ro[i] = next_row(ro[(i + NB - 1) % NB]); K[i] = bld<NT_>(kr, ko, ro[i]); SB;
    }
    const u32 pp = softmax_step(d, c & 3, a);
    SB;
    a.acc = vecmat(V[0], dppu<BC0>(pp), sel16, a.acc); SB; V[0] = bld<NT_>(vr, vo, ro[0]); SB;
    a.acc = vecmat(V[1], dppu<BC1>(pp), sel16, a.acc); SB; V[1] = bld<NT_>(vr, vo, ro[1]); SB;
    a.acc = vecmat(V[2], dppu<BC2>(pp), sel16, a.acc); SB; V[2] = bld<NT_>(vr, vo, ro[2]); SB;
    a.acc = vecmat(V[3], dppu<BC3>(pp), sel16, a.acc); SB; V[3] = bld<NT_>(vr, vo, ro[3]); SB;
  };
  auto probs = [&](const u32x4 (&X)[NB]) {
#pragma unroll
    for (int i = 0; i < NB; ++i) d[i] = mm(X[i], q, zero4);
    return softmax_step(d, c & 3, a);
  };
  auto pv = [&](const u32x4 (&X)[NB], u32 pp) {
    a.acc = vecmat(X[0], dppu<BC0>(pp), sel16, a.acc); a.acc = vecmat(X[1], dppu<BC1>(pp), sel16, a.acc);
    a.acc = vecmat(X[2], dppu<BC2>(pp), sel16, a.acc); a.acc = vecmat(X[3], dppu<BC3>(pp), sel16, a.acc);
  };
  ring_step();
  pv(VB, probs(KB)); SB;
  pv(VC, probs(KC)); SB;
  for (int t = 4; t < NBT; ++t) {
    ring_step();
    __builtin_amdgcn_s_barrier();
    SB;
  }

  const u32 toff = (w == 1 && lane == 0) ? WS_TICKET : OOB;
  const u32 ticket = __builtin_amdgcn_raw_ptr_buffer_atomic_add_i32(1, wsr, toff, 0, 0);
  SB;
  u32 pp = probs(K);
  u32 mlr = 2 * (lane >> 3), opr = (lane >> 3) * DH + acc_pos(16 * w + 2 * (lane & 7)), opw = w * DH + (r * 8 + c) * 4;
  u32 pub = WS_H1 + (h * NSPLIT + s) * REC1 + (lane < 8 ? (8 * w + lane) * 16 : 1024);
  pub = (lane < 8 || (lane == 8 && w == 0)) ? pub : OOB;
  {
    float L = a.l + dpp<XOR1>(a.l);
    L = xrow_sum(L + dpp<XOR2>(L));
    if (lane == 0) *reinterpret_cast<f32x2*>(ml_s + 2 * w) = f32x2{a.m, L};
  }
  asm volatile("" : "+v"(pub), "+v"(mlr), "+v"(opr), "+v"(opw), "+v"(pp));
  SB;
  pv(V, pp);
  if (c < 8) *reinterpret_cast<f32x4*>(op_s + opw) = a.acc;
  if (w == 1) {
    auto valid = [&](u32 v) { return (v >> 12) == (tag & 0xfffffu) && (v & 0xfffu) >= TICKET0; };
    u32 tk = __builtin_amdgcn_readfirstlane(ticket);
    if (!valid(tk)) {
#pragma nounroll
      for (u32 spin = 0; spin < SPIN_CAP; ++spin) {
        __builtin_amdgcn_s_sleep(8);
        asm volatile("" ::: "memory");
        if (valid(__builtin_amdgcn_readfirstlane(__builtin_amdgcn_raw_buffer_load_b32(wsr, toff, 0, SC1)))) break;
      }
      tk = __builtin_amdgcn_readfirstlane(__builtin_amdgcn_raw_ptr_buffer_atomic_add_i32(1, wsr, toff, 0, 0));
    }
    if (lane == 0) *role_s = (tk & 0xfffu) - TICKET0;
  }
  __syncthreads();
  const u32 role = __builtin_amdgcn_readfirstlane(*role_s);
  {
    const f32x2 ml = *reinterpret_cast<const f32x2*>(ml_s + mlr);
    const float M = oct_max(ml[0]), f = __builtin_amdgcn_exp2f(ml[0] - M);
    float o0 = f * op_s[opr], o1 = f * op_s[opr + 4], L = f * ml[1];
    o0 += dpp<ROR8>(o0); o1 += dpp<ROR8>(o1); L += dpp<ROR8>(L);
    o0 = fold32(fold16(o0, o1));
    L = xrow_sum(L);
    o1 = asf(__builtin_amdgcn_permlane16_swap(asu(o0), asu(o0), false, false)[1]);
    const bool last = lane == 8;
    st_unit(wsr, pub, last ? M : o0, last ? L : o1, tag);
  }

  if (role < NTILE) {
    const int hp = role / NSL, sp = role % NSL;
    u32x4 WO[4], WD[2];
    u32x2 HG[2];
    u32 hoff[2];
    {
      const rsrc_t wor = mkrsrc(wo, NH * DH * DM * 2), wdr = mkrsrc(wd, FF * DM * 2);
      const u32 o = (u32)((hp * DH + 16 * w + 4 * r) * DM + sp * 128 + 8 * c) * 2;
#pragma unroll
      for (int i = 0; i < 2; ++i) {
        const int fr = 8 * w + 4 * i + r, f = hp * FROWS + (fr < FROWS ? fr : 0);
        hoff[i] = WS_HG + ((f >> 6) * 8 + (c & 7)) * 1024 + (c >> 3) * 512 + (f & 63) * 8;
        HG[i] = ld_gran(wsr, hoff[i]);
        WD[i] = bld<NT_>(wdr, fr < FROWS ? (u32)(f * DM + sp * 128 + 8 * c) * 2 : OOB, 0);
      }
#pragma unroll
      for (int i = 0; i < 4; ++i) WO[i] = bld<NT_>(wor, o + i * DM * 2, 0);
      SB;
    }
    if (!__all(HG[0][1] == tag && HG[1][1] == tag)) {
#pragma nounroll
      for (u32 spin = 0; spin < SPIN_CAP; ++spin) {
        asm volatile("" ::: "memory");
#pragma unroll
        for (int i = 0; i < 2; ++i) HG[i] = ld_gran(wsr, hoff[i]);
        if (__all(HG[0][1] == tag && HG[1][1] == tag)) break;
        __builtin_amdgcn_s_sleep(1);
      }
    }
    f32x4 y = zero4;
#pragma unroll
    for (int i = 0; i < 2; ++i) {
      float t = asf(HG[i][0]);
      t += dpp<XOR1>(t); t += dpp<XOR2>(t); t += dpp<HMIR>(t);
      const float o = dpp<ROR8>(t);
      const float g = rbf(c < 8 ? t : o), u = rbf(c < 8 ? o : t);
      const float hv = rbf(g * __builtin_amdgcn_rcpf(1.f + __expf(-g))) * u;
      y = vecmat(WD[i], pk_bf16(hv, hv), sel16, y);
    }
    const u32 rec = WS_H1 + (hp * NSPLIT + (c >> 1)) * REC1;
    const u32 oa = rec + (8 * w + 2 * r + (c & 1)) * 16, om = rec + 1024;
    const u32 yc = 16 * w + 2 * (lane & 7);
    u32 ypw = w * 128 + (r * 8 + c) * 4, ypr = (lane >> 3) * 128 + acc_pos(yc);
    u32 pub2 = lane < 8 ? WS_H2 + (sp * NH + hp) * 1024 + yc * 8 : OOB;
    asm volatile("" : "+v"(pub2), "+v"(ypw), "+v"(ypr));
    u32 a0, a1, m0, m1;
    {
      const u32x4 na = ld_unit(wsr, oa), nm = ld_unit(wsr, om);
      a0 = na[0]; a1 = na[2]; m0 = nm[0]; m1 = nm[2];
      const u64 need = __ballot(!(fresh(na, tag) && fresh(nm, tag)));
      if (need) poll_records(wsr, oa, om, tag, need, a0, a1, m0, m1);
    }
    float M = asf(m0);
    M = fmaxf(M, dpp<XOR2>(M)); M = fmaxf(M, dpp<ROR4>(M)); M = fmaxf(M, dpp<ROR8>(M));
    const float f = __builtin_amdgcn_exp2f(asf(m0) - M);
    float L = f * asf(m1), o0 = f * asf(a0), o1 = f * asf(a1);
    L += dpp<XOR2>(L); o0 += dpp<XOR2>(o0); o1 += dpp<XOR2>(o1);
    L += dpp<ROR4>(L); o0 += dpp<ROR4>(o0); o1 += dpp<ROR4>(o1);
    L += dpp<ROR8>(L); o0 += dpp<ROR8>(o0); o1 += dpp<ROR8>(o1);
    const float il = __builtin_amdgcn_rcpf(L);
    o0 *= il; o1 *= il;
    const u32 e0 = pk_bf16(o0, o0), e1 = pk_bf16(o1, o1), n0 = dppu<XOR1>(e0), n1 = dppu<XOR1>(e1);
    const bool odd = c & 1;
    const u32x4 b0 = diag(odd ? n0 : e0, sel16), b1 = diag(odd ? n1 : e1, sel16);
    const u32x4 b2 = diag(odd ? e0 : n0, sel16), b3 = diag(odd ? e1 : n1, sel16);
    f32x4 y2 = mm(WO[1], b1, zero4);
    y = mm(WO[0], b0, y); y2 = mm(WO[3], b3, y2); y = mm(WO[2], b2, y);
    if (c < 8) *reinterpret_cast<f32x4*>(yp_s + ypw) = y + y2;
    asm volatile("s_waitcnt lgkmcnt(0)\n\ts_barrier" ::: "memory");
    st_unit(wsr, pub2, oct_sum(yp_s[ypr]), oct_sum(yp_s[ypr + 4]), tag);
    asm volatile("s_waitcnt vmcnt(0)" ::: "memory");
  } else if (role < NTILE + NRED) {
    const int col = ((role - NTILE) * NW + w) * 4 + 2 * (lane & 1);
    const u32 off = WS_H2 + ((col >> 7) * NH + (lane >> 1)) * 1024 + (col & 127) * 8;
    const u32 xw = x32[col >> 1];
    u32 u0, u1;
    {
      const u32x4 n = ld_unit(wsr, off);
      u0 = n[0]; u1 = n[2];
      const u64 need = __ballot(!fresh(n, tag));
      asm volatile("" :: "v"(xw));
      if (need) poll_units(wsr, off, tag, need, u0, u1);
    }
    float y0 = asf(u0), y1 = asf(u1);
    y0 += dpp<XOR2>(y0); y1 += dpp<XOR2>(y1);
    y0 += dpp<ROR4>(y0); y1 += dpp<ROR4>(y1);
    y0 = oct_sum(y0); y1 = oct_sum(y1);
    if (lane < 2) out32[col >> 1] = pk_bf16(lo16(xw) + y0, hi16(xw) + y1);
    if (role == NTILE && w == 0 && lane == 0)
      __hip_atomic_store((GAS u32*)(ws + WS_EPOCH), tag, __ATOMIC_RELAXED, __HIP_MEMORY_SCOPE_AGENT);
    asm volatile("s_waitcnt vmcnt(0)" ::: "memory");
  }
}

}

extern "C" void kernel_launch(void* const* d_in, const int* in_sizes, int n_in, void* d_out, int out_size,
                              void* d_ws, size_t ws_size, hipStream_t stream) {
  (void)in_sizes; (void)n_in; (void)out_size; (void)ws_size;
  fused_decode<<<NWG, NT, 0, stream>>>((const u32*)d_in[0], (const char*)d_in[1], (const char*)d_in[2],
                                       (const char*)d_in[3], (const char*)d_in[4], (const char*)d_in[6],
                                       (const char*)d_in[7], (const char*)d_in[8], (u32*)d_out, (char*)d_ws);
}
```

```cpp
#include <hip/hip_runtime.h>
#include <stdint.h>

namespace {

typedef unsigned u32;
typedef unsigned long long u64;
typedef u32 u32x4 __attribute__((ext_vector_type(4)));
typedef u32 u32x2 __attribute__((ext_vector_type(2)));
typedef float f32x2 __attribute__((ext_vector_type(2)));
typedef float f32x4 __attribute__((ext_vector_type(4)));
typedef __bf16 bf16x2 __attribute__((ext_vector_type(2)));
typedef __bf16 bf16x8 __attribute__((ext_vector_type(8)));
typedef __amdgpu_buffer_rsrc_t rsrc_t;
#define GAS __attribute__((address_space(1)))
#define CAS __attribute__((address_space(4)))
#define SB __builtin_amdgcn_sched_barrier(0)

constexpr int DM = 640, NH = 32, SK = 16384, DH = 128, FF = 1728;
constexpr int NSPLIT = 8, NWG = NH * NSPLIT, NW = 8, NT = NW * 64;
constexpr int KEYS_WG = SK / NSPLIT;
constexpr u32 KV_BYTES = KEYS_WG * DH * 2, ROW = NW * 1024;
constexpr int NB = 4, NBT = KV_BYTES / ROW / NB;
constexpr int NROW = NB * NBT;
constexpr int RING = 4;
constexpr int G1 = 4, G2 = 12, G3 = 20, NPRE = 28;
constexpr int QROWS = DM / NSPLIT;
constexpr int NSL = 5;
constexpr int FROWS = FF / NH;
constexpr int FLINES = FF / 64;
constexpr u32 NTILE = NH * NSL, NRED = DM / (4 * NW);
constexpr u32 OOB = 0x7ff00000u;
constexpr int NT_ = 2, SC1 = 16;

constexpr u32 WS_EPOCH = 0;
constexpr u32 WS_TICKET = 2048;
constexpr u32 WS_QP = 4096;
constexpr u32 WS_HG = WS_QP + NWG * 1024;
constexpr u32 REC1 = 1280;
constexpr u32 WS_H1 = WS_HG + FLINES * 8 * 1024;
constexpr u32 WS_H2 = WS_H1 + NWG * REC1;
constexpr u32 WS_END = WS_H2 + NSL * NH * 1024;
constexpr u32 TICKET0 = 0x400;
constexpr u32 SPIN_CAP = 1u << 18;

constexpr float LOG2E = 1.4426950408889634f;
constexpr float SCALE = 0.08838834764831845f;

__device__ __forceinline__ float asf(u32 x) { return __builtin_bit_cast(float, x); }
__device__ __forceinline__ u32 asu(float x) { return __builtin_bit_cast(u32, x); }
__device__ __forceinline__ float lo16(u32 x) { return asf(x << 16); }
__device__ __forceinline__ float hi16(u32 x) { return asf(x & 0xffff0000u); }
__device__ __forceinline__ u32 pk_bf16(float a, float b) { bf16x2 v = {(__bf16)a, (__bf16)b}; return __builtin_bit_cast(u32, v); }
__device__ __forceinline__ float rbf(float x) { return (float)(__bf16)x; }

template <int CTRL> __device__ __forceinline__ u32 dppu(u32 x) { return __builtin_amdgcn_mov_dpp(x, CTRL, 0xf, 0xf, true); }
template <int CTRL> __device__ __forceinline__ float dpp(float x) { return asf(dppu<CTRL>(asu(x))); }
constexpr int XOR1 = 0xB1, XOR2 = 0x4E, HMIR = 0x141, ROR4 = 0x124, ROR8 = 0x128;
constexpr int BC0 = 0x00, BC1 = 0x55, BC2 = 0xAA, BC3 = 0xFF;
__device__ __forceinline__ float row_sum(float x) {
  x += dpp<XOR1>(x); x += dpp<XOR2>(x); x += dpp<ROR4>(x); x += dpp<ROR8>(x); return x;
}
__device__ __forceinline__ float fold16(float a, float b) {
  auto s = __builtin_amdgcn_permlane16_swap(asu(a), asu(b), false, false);
  return asf(s[0]) + asf(s[1]);
}
__device__ __forceinline__ float fold32(float x) {
  auto s = __builtin_amdgcn_permlane32_swap(asu(x), asu(x), false, false);
  return asf(s[0]) + asf(s[1]);
}
__device__ __forceinline__ float xrow_sum(float x) { return fold32(fold16(x, x)); }
__device__ __forceinline__ float xrow_max(float x) {
  auto s = __builtin_amdgcn_permlane16_swap(asu(x), asu(x), false, false);
  x = fmaxf(asf(s[0]), asf(s[1]));
  auto t = __builtin_amdgcn_permlane32_swap(asu(x), asu(x), false, false);
  return fmaxf(asf(t[0]), asf(t[1]));
}
__device__ __forceinline__ float oct_sum(float x) { return xrow_sum(x + dpp<ROR8>(x)); }
__device__ __forceinline__ float oct_max(float x) { return xrow_max(fmaxf(x, dpp<ROR8>(x))); }

__device__ __forceinline__ f32x4 mm(u32x4 a, u32x4 b, f32x4 acc) {
  return __builtin_amdgcn_mfma_f32_16x16x32_bf16(__builtin_bit_cast(bf16x8, a), __builtin_bit_cast(bf16x8, b), acc, 0, 0, 0);
}
__device__ __forceinline__ u32x4 diag_sel(int j) {
  const u32 half = (j & 1) ? 0xffff0000u : 0x0000ffffu;
  return u32x4{(j >> 1) == 0 ? half : 0u, (j >> 1) == 1 ? half : 0u, (j >> 1) == 2 ? half : 0u, (j >> 1) == 3 ? half : 0u};
}
__device__ __forceinline__ u32x4 diag(u32 xx, u32x4 sel) { return u32x4{xx & sel[0], xx & sel[1], xx & sel[2], xx & sel[3]}; }
__device__ __forceinline__ f32x4 vecmat(u32x4 w, u32 xx, u32x4 sel, f32x4 acc) { return mm(w, diag(xx, sel), acc); }
__device__ __forceinline__ u32 acc_pos(u32 e) { return ((e >> 5) * 8 + (e & 7)) * 4 + ((e >> 3) & 3); }

__device__ __forceinline__ rsrc_t mkrsrc(const void* p, u32 bytes) {
  return __builtin_amdgcn_make_buffer_rsrc(const_cast<void*>(p), 0, bytes, 0x00020000);
}
template <int AUX> __device__ __forceinline__ u32x4 bld(rsrc_t r, u32 voff, u32 soff) {
  return __builtin_amdgcn_raw_buffer_load_b128(r, voff, soff, AUX);
}
__device__ __forceinline__ void st_unit(rsrc_t r, u32 off, float a, float b, u32 tag) {
  __builtin_amdgcn_raw_buffer_store_b128(u32x4{asu(a), tag, asu(b), tag}, r, off, 0, SC1);
}
__device__ __forceinline__ u32x4 ld_unit(rsrc_t r, u32 off) { return bld<SC1>(r, off, 0); }
__device__ __forceinline__ u32x2 ld_gran(rsrc_t r, u32 off) { return __builtin_amdgcn_raw_buffer_load_b64(r, off, 0, SC1); }
__device__ __forceinline__ bool fresh(u32x4 u, u32 tag) { return u[1] == tag && u[3] == tag; }

#define POLL_GAP "s_sleep 5\n\t"
#define POLL_TAIL(SLP, L)                                   \
  "s_andn2_b64 %[need], %[need], vcc\n\t"                   \
  "s_cbranch_scc0 2f\n\t"                                   \
  "s_bcnt1_i32_b64 %[n], %[need]\n\t"                       \
  "s_cmp_le_u32 %[n], 8\n\t"                                \
  "s_cbranch_scc1 " L "f\n\t"                               \
  "s_sleep " SLP "\n"                                       \
  L ":\n\t"                                                 \
  "s_mov_b64 exec, %[need]\n\t"
#define POLL_LOOP_END                                       \
  "s_sub_u32 %[cnt], %[cnt], 1\n\t"                         \
  "s_cbranch_scc0 1b\n"                                     \
  "2:\n\t"                                                  \
  "s_nop 1"
#define POLL2_LD(A) "buffer_load_dwordx4 v[" #A ":" #A "+3], %[o], %[rs], 0 offen sc1\n\t"
#define POLL2_PASS(A, L)                                    \
  "s_waitcnt vmcnt(2)\n\t"                                  \
  "v_cmp_eq_u32 vcc, %[tag], v[" #A "+1]\n\t"               \
  "v_cmp_eq_u32 %[t], %[tag], v[" #A "+3]\n\t"              \
  "s_and_b64 vcc, vcc, %[t]\n\t"                            \
  "s_and_b64 vcc, vcc, %[need]\n\t"                         \
  "v_cndmask_b32 %[u0], %[u0], v[" #A "], vcc\n\t"          \
  "v_cndmask_b32 %[u1], %[u1], v[" #A "+2], vcc\n\t"        \
  POLL_TAIL("8", L) POLL2_LD(A) "s_mov_b64 exec, -1\n\t"
__device__ __forceinline__ void poll_units(rsrc_t rs, u32 o, u32 tag, u64 need, u32& u0, u32& u1) {
  u64 t; u32 n, cnt = SPIN_CAP;
  asm volatile(
      "s_mov_b64 exec, %[need]\n\t" POLL2_LD(208) POLL_GAP POLL2_LD(212) POLL_GAP POLL2_LD(216) "s_mov_b64 exec, -1\n"
      "1:\n\t" POLL2_PASS(208, "3") POLL2_PASS(212, "4") POLL2_PASS(216, "5") POLL_LOOP_END
      : [u0] "+v"(u0), [u1] "+v"(u1), [need] "+s"(need), [cnt] "+s"(cnt), [t] "=&s"(t), [n] "=&s"(n)
      : [o] "v"(o), [rs] "s"(rs), [tag] "s"(tag)
      : "vcc", "scc", "memory", "v208", "v209", "v210", "v211", "v212", "v213", "v214", "v215", "v216", "v217", "v218", "v219");
}
#define POLL1_LD(A)                                                              \
  "buffer_load_dwordx4 v[" #A ":" #A "+3], %[oa], %[rs], 0 offen sc1\n\t"        \
  "buffer_load_dwordx4 v[" #A "+4:" #A "+7], %[om], %[rs], 0 offen sc1\n\t"
#define POLL1_PASS(A, L)                                    \
  "s_waitcnt vmcnt(4)\n\t"                                  \
  "v_cmp_eq_u32 vcc, %[tag], v[" #A "+1]\n\t"               \
  "v_cmp_eq_u32 %[t], %[tag], v[" #A "+3]\n\t"              \
  "s_and_b64 %[g], vcc, %[t]\n\t"                           \
  "v_cmp_eq_u32 vcc, %[tag], v[" #A "+5]\n\t"               \
  "v_cmp_eq_u32 %[t], %[tag], v[" #A "+7]\n\t"              \
  "s_and_b64 %[g], %[g], %[need]\n\t"                       \
  "s_and_b64 vcc, vcc, %[t]\n\t"                            \
  "s_and_b64 vcc, vcc, %[g]\n\t"                            \
  "v_cndmask_b32 %[a0], %[a0], v[" #A "], vcc\n\t"          \
  "v_cndmask_b32 %[a1], %[a1], v[" #A "+2], vcc\n\t"        \
  "v_cndmask_b32 %[m0], %[m0], v[" #A "+4], vcc\n\t"        \
  "v_cndmask_b32 %[m1], %[m1], v[" #A "+6], vcc\n\t"        \
  POLL_TAIL("2", L) POLL1_LD(A) "s_mov_b64 exec, -1\n\t"
__device__ __forceinline__ void poll_records(rsrc_t rs, u32 oa, u32 om, u32 tag, u64 need, u32& a0, u32& a1, u32& m0, u32& m1) {
  u64 t, g; u32 n, cnt = SPIN_CAP;
  asm volatile(
      "s_mov_b64 exec, %[need]\n\t" POLL1_LD(208) POLL_GAP POLL1_LD(216) POLL_GAP POLL1_LD(224) "s_mov_b64 exec, -1\n"
      "1:\n\t" POLL1_PASS(208, "3") POLL1_PASS(216, "4") POLL1_PASS(224, "5") POLL_LOOP_END
      : [a0] "+v"(a0), [a1] "+v"(a1), [m0] "+v"(m0), [m1] "+v"(m1), [need] "+s"(need), [cnt] "+s"(cnt), [t] "=&s"(t),
        [g] "=&s"(g), [n] "=&s"(n)
      : [oa] "v"(oa), [om] "v"(om), [rs] "s"(rs), [tag] "s"(tag)
      : "vcc", "scc", "memory", "v208", "v209", "v210", "v211", "v212", "v213", "v214", "v215", "v216", "v217", "v218", "v219",
        "v220", "v221", "v222", "v223", "v224", "v225", "v226", "v227", "v228", "v229", "v230", "v231");
}

__device__ __forceinline__ float quad_trace(f32x4 d) {
  float t = ((dpp<BC0>(d[0]) + dpp<BC1>(d[1])) + dpp<BC2>(d[2])) + dpp<BC3>(d[3]);
  asm("" : "+v"(t));
  return t;
}
__device__ __forceinline__ float batch_score(const f32x4 (&d)[NB], int slot) {
  const float t0 = quad_trace(d[0]), t1 = quad_trace(d[1]), t2 = quad_trace(d[2]), t3 = quad_trace(d[3]);
  const float t = (slot & 2) ? ((slot & 1) ? t3 : t2) : ((slot & 1) ? t1 : t0);
  return rbf(rbf(t) * SCALE) * LOG2E;
}
template <int I> __device__ __forceinline__ u32 quad_bc(u32 x) { return dppu<I == 0 ? BC0 : I == 1 ? BC1 : I == 2 ? BC2 : BC3>(x); }

__global__ void __launch_bounds__(NT, 2)
fused_decode(const u32* __restrict__ x32, const char* __restrict__ kc, const char* __restrict__ vc,
             const char* __restrict__ wq, const char* __restrict__ wo, const char* __restrict__ wg,
             const char* __restrict__ wu, const char* __restrict__ wd, u32* __restrict__ out32, char* ws) {
  __shared__ __attribute__((aligned(16))) float lds[5 * NW * 128 + 64 + 2 * NW + 4];
  u32* xn_s = reinterpret_cast<u32*>(lds);
  float* qp_s = lds + NW * 128;
  float* fp_s = qp_s + NW * 128;
  float* op_s = fp_s + NW * 128;
  float* yp_s = op_s + NW * 128;
  u32* q_s = reinterpret_cast<u32*>(yp_s + NW * 128);
  float* ml_s = yp_s + NW * 128 + 64;
  u32* role_s = reinterpret_cast<u32*>(ml_s + 2 * NW);

  asm volatile("" :: "s"(x32), "s"(kc), "s"(vc), "s"(wq), "s"(wo), "s"(wg), "s"(wu), "s"(wd), "s"(out32), "s"(ws));
  const int tid = threadIdx.x, lane = tid & 63, r = lane >> 4, c = lane & 15;
  const int w = __builtin_amdgcn_readfirstlane(tid >> 6);
  const int b = blockIdx.x, xs = b & 7, j = b >> 3;
  const int h = xs * 4 + (j >> 3), s = j & 7;
  const rsrc_t wsr = mkrsrc(ws, WS_END);
  const f32x4 zero4 = {0.f, 0.f, 0.f, 0.f};

  const int qrow = s * QROWS + 4 * w;
  const bool ffn_on = b < FLINES * 8;
  const int frow = xs * 80 + 8 * (w & 3);
  const size_t kv0 = (size_t)(h * NSPLIT + s) * KV_BYTES;
  const rsrc_t kr = mkrsrc(kc + kv0, KV_BYTES), vr = mkrsrc(vc + kv0, KV_BYTES);
  const u32 vo = lane * 16 + w * 1024;
  const u32 ko = (c >> 2) * 256 + (c & 3) * 64 + r * 16 + w * 1024;
  const u32 row0 = (u32)(h * NSPLIT + s) * ROW;
  u32x4 L[2 * NROW];
  auto request = [&](int n) {
    const u32 o = (row0 + (n % NROW) * ROW) & (KV_BYTES - 1);
    L[n] = n < NROW ? bld<NT_>(kr, ko, o) : bld<NT_>(vr, vo, o);
  };
  auto request_range = [&](int n0, int n1) {
#pragma unroll
    for (int n = n0; n < n1; ++n) request(n);
    SB;
  };
  const rsrc_t xrs = mkrsrc(x32, DM * 2);
  const u32x4 xa = bld<0>(xrs, lane * 16, 0), xb = bld<0>(xrs, lane * 16 + 1024, 0);
  u32 tag = *(const CAS u32*)(ws + WS_EPOCH) + 1u;
  u32x4 WQ[3];
  {
    const rsrc_t qr = mkrsrc(wq, DM * NH * DH * 2);
    const u32 o = (u32)((qrow + r) * (NH * DH) + h * DH + 8 * c) * 2;
    WQ[0] = bld<NT_>(qr, o, 0);
    WQ[1] = bld<NT_>(qr, o + 32 * NH * DH * 2, 0);
    WQ[2] = bld<NT_>(qr, w < 4 ? o + 64 * NH * DH * 2 : OOB, 0);
    SB;
  }
  request_range(0, G1);

  {
    float xv[16], sum = 0.f;
#pragma unroll
    for (int i = 0; i < 4; ++i) { xv[2 * i] = lo16(xa[i]); xv[2 * i + 1] = hi16(xa[i]); xv[8 + 2 * i] = lo16(xb[i]); xv[9 + 2 * i] = hi16(xb[i]); }
#pragma unroll
    for (int i = 0; i < 16; ++i) sum += xv[i];
    const float mean = xrow_sum(row_sum(sum)) * (1.f / DM);
    float sq = 0.f, sq2 = 0.f;
#pragma unroll
    for (int i = 0; i < 8; ++i) { const float d = xv[i] - mean, e = xv[8 + i] - mean; sq = fmaf(d, d, sq); sq2 = fmaf(e, e, sq2); }
    const float var = xrow_sum(row_sum(lane < 16 ? sq + sq2 : sq)) * (1.f / DM);
    const float mb = rbf(mean);
    const float inv = __builtin_amdgcn_rcpf(rbf(__builtin_amdgcn_sqrtf(rbf(rbf(var) + 1e-5f))));
    u32 n[16];
#pragma unroll
    for (int i = 0; i < 16; ++i) { const float t = rbf(xv[i] - mb) * inv; n[i] = pk_bf16(t, t); }
    *reinterpret_cast<u32x4*>(xn_s + 8 * lane) = u32x4{n[0], n[1], n[2], n[3]};
    *reinterpret_cast<u32x4*>(xn_s + 8 * lane + 4) = u32x4{n[4], n[5], n[6], n[7]};
    *reinterpret_cast<u32x4*>(xn_s + 512 + 8 * lane) = u32x4{n[8], n[9], n[10], n[11]};
    *reinterpret_cast<u32x4*>(xn_s + 512 + 8 * lane + 4) = u32x4{n[12], n[13], n[14], n[15]};
  }
  SB;
  request_range(G1, G2);

  const u32x4 sel16 = diag_sel(c);
  {
    f32x4 a = zero4;
#pragma unroll
    for (int i = 0; i < 3; ++i) a = vecmat(WQ[i], xn_s[qrow + 32 * i + r], sel16, a);
    if (c < 8) *reinterpret_cast<f32x4*>(qp_s + w * 128 + (r * 8 + c) * 4) = a;
  }
  asm volatile("" : "+s"(tag));
  __syncthreads();
  if (w == 0) {
    const float* p = qp_s + acc_pos(2 * lane);
    float t0 = 0.f, t1 = 0.f;
#pragma unroll
    for (int i = 0; i < NW; ++i) { t0 += p[i * 128]; t1 += p[i * 128 + 4]; }
    st_unit(wsr, WS_QP + (h * NSPLIT + s) * 1024 + lane * 16, t0, t1, tag);
    if (b == 0 && lane == 0)
      __hip_atomic_store((GAS u32*)(ws + WS_TICKET), (tag << 12) + TICKET0, __ATOMIC_RELAXED, __HIP_MEMORY_SCOPE_AGENT);
  }
  SB;

  u32x4 WF[3];
  {
    const rsrc_t fr = mkrsrc(w < 4 ? wg : wu, ffn_on ? DM * FF * 2 : 0);
    const u32 o = (u32)((frow + (lane >> 3)) * FF + j * 64 + 8 * (lane & 7)) * 2;
    WF[0] = bld<NT_>(fr, o, 0);
    WF[1] = bld<NT_>(fr, o + 32 * FF * 2, 0);
    WF[2] = bld<NT_>(fr, (w & 3) < 2 ? o + 64 * FF * 2 : OOB, 0);
    SB;
  }
  request_range(G2, G3);
  const u32 qoff = WS_QP + h * NSPLIT * 1024 + lane * 16;
  u32x4 qu[NSPLIT];
  if (w == 0) {
#pragma unroll
    for (int i = 0; i < NSPLIT; ++i) qu[i] = ld_unit(wsr, qoff + i * 1024);
  }
  SB;
  request_range(G3, NPRE);
  if (w == 0) {
    auto all_fresh = [&]() {
      bool ok = true;
#pragma unroll
      for (int i = 0; i < NSPLIT; ++i) ok = ok && fresh(qu[i], tag);
      return __all(ok);
    };
    if (!all_fresh()) {
#pragma nounroll
      for (u32 spin = 0; spin < SPIN_CAP; ++spin) {
        asm volatile("" ::: "memory");
#pragma unroll
        for (int i = 0; i < NSPLIT; ++i) qu[i] = ld_unit(wsr, qoff + i * 1024);
        if (all_fresh()) break;
      }
    }
    float t0 = 0.f, t1 = 0.f;
#pragma unroll
    for (int i = 0; i < NSPLIT; ++i) { t0 += asf(qu[i][0]); t1 += asf(qu[i][2]); }
    q_s[lane] = pk_bf16(t0, t1);
  }
  {
    const u32x4 sel8 = diag_sel(lane & 7);
    f32x4 f = zero4;
#pragma unroll
    for (int i = 0; i < 3; ++i) f = vecmat(WF[i], xn_s[frow + 32 * i + (lane >> 3)], sel8, f);
    if ((c >> 3) == (r >> 1)) *reinterpret_cast<f32x4*>(fp_s + w * 128 + (r * 8 + (c & 7)) * 4) = f;
  }
  __syncthreads();
  const u32x4 q = *reinterpret_cast<const u32x4*>(q_s + 16 * (c & 3) + 4 * r);
  if (w == NW - 1 && ffn_on) {
    const float* p = fp_s + (lane >> 5) * 512 + acc_pos(2 * (lane & 31));
    float g0 = 0.f, g1 = 0.f;
#pragma unroll
    for (int i = 0; i < 8; ++i) { g0 += p[i * 64]; g1 += p[i * 64 + 4]; }
    st_unit(wsr, WS_HG + b * 1024 + lane * 16, g0, g1, tag);
  }

  auto refill = [&](int n) {
    const int nx = n < RING ? NPRE + n : n >= NPRE ? n + RING : 2 * NROW;
    if (nx < 2 * NROW) { SB; request(nx); SB; }
  };
  float sv[NBT];
#pragma unroll
  for (int t = 0; t < NBT; ++t) {
    f32x4 d[NB];
#pragma unroll
    for (int i = 0; i < NB; ++i) { d[i] = mm(L[NB * t + i], q, zero4); refill(NB * t + i); }
    sv[t] = batch_score(d, c & 3);
    asm volatile("" : "+v"(sv[t]));
  }
  float mx = sv[0];
#pragma unroll
  for (int t = 1; t < NBT; ++t) mx = fmaxf(mx, sv[t]);
  mx = fmaxf(mx, dpp<XOR1>(mx));
  mx = xrow_max(fmaxf(mx, dpp<XOR2>(mx)));
  u32 pp[NBT];
  float ls = 0.f;
#pragma unroll
  for (int t = 0; t < NBT; ++t) { const float p = __builtin_amdgcn_exp2f(sv[t] - mx); ls += p; pp[t] = pk_bf16(p, p); }
  ls += dpp<XOR1>(ls);
  ls = xrow_sum(ls + dpp<XOR2>(ls));
  if (lane == 0) *reinterpret_cast<f32x2*>(ml_s + 2 * w) = f32x2{mx, ls};
  const u32 toff = (w == 1 && lane == 0) ? WS_TICKET : OOB;
  u32 ticket = 0;
  f32x4 acc = zero4;
#pragma unroll
  for (int t = 0; t < NBT - 1; ++t) {
    if (t == NBT - 2) { SB; ticket = __builtin_amdgcn_raw_ptr_buffer_atomic_add_i32(1, wsr, toff, 0, 0); SB; }
    const int n = NROW + NB * t;
    acc = vecmat(L[n], quad_bc<0>(pp[t]), sel16, acc); refill(n);
    acc = vecmat(L[n + 1], quad_bc<1>(pp[t]), sel16, acc); refill(n + 1);
    acc = vecmat(L[n + 2], quad_bc<2>(pp[t]), sel16, acc); refill(n + 2);
    acc = vecmat(L[n + 3], quad_bc<3>(pp[t]), sel16, acc); refill(n + 3);
  }
  u32 mlr = 2 * (lane >> 3), opr = (lane >> 3) * DH + acc_pos(16 * w + 2 * (lane & 7)), opw = w * DH + (r * 8 + c) * 4;
  u32 pub = WS_H1 + (h * NSPLIT + s) * REC1 + (lane < 8 ? (8 * w + lane) * 16 : 1024);
  pub = (lane < 8 || (lane == 8 && w == 0)) ? pub : OOB;
  asm volatile("" : "+v"(pub), "+v"(mlr), "+v"(opr), "+v"(opw));
  SB;
  {
    constexpr int n = 2 * NROW - NB;
    acc = vecmat(L[n], quad_bc<0>(pp[NBT - 1]), sel16, acc); acc = vecmat(L[n + 1], quad_bc<1>(pp[NBT - 1]), sel16, acc);
    acc = vecmat(L[n + 2], quad_bc<2>(pp[NBT - 1]), sel16, acc); acc = vecmat(L[n + 3], quad_bc<3>(pp[NBT - 1]), sel16, acc);
  }
  if (c < 8) *reinterpret_cast<f32x4*>(op_s + opw) = acc;
  if (w == 1) {
    auto valid = [&](u32 v) { return (v >> 12) == (tag & 0xfffffu) && (v & 0xfffu) >= TICKET0; };
    u32 tk = __builtin_amdgcn_readfirstlane(ticket);
    if (!valid(tk)) {
#pragma nounroll
      for (u32 spin = 0; spin < SPIN_CAP; ++spin) {
        __builtin_amdgcn_s_sleep(8);
        asm volatile("" ::: "memory");
        if (valid(__builtin_amdgcn_readfirstlane(__builtin_amdgcn_raw_buffer_load_b32(wsr, toff, 0, SC1)))) break;
      }
      tk = __builtin_amdgcn_readfirstlane(__builtin_amdgcn_raw_ptr_buffer_atomic_add_i32(1, wsr, toff, 0, 0));
    }
    if (lane == 0) *role_s = (tk & 0xfffu) - TICKET0;
  }
  __syncthreads();
  const u32 role = __builtin_amdgcn_readfirstlane(*role_s);
  {
    const f32x2 ml = *reinterpret_cast<const f32x2*>(ml_s + mlr);
    const float M = oct_max(ml[0]), f = __builtin_amdgcn_exp2f(ml[0] - M);
    float o0 = f * op_s[opr], o1 = f * op_s[opr + 4], L = f * ml[1];
    o0 += dpp<ROR8>(o0); o1 += dpp<ROR8>(o1); L += dpp<ROR8>(L);
    o0 = fold32(fold16(o0, o1));
    L = xrow_sum(L);
    o1 = asf(__builtin_amdgcn_permlane16_swap(asu(o0), asu(o0), false, false)[1]);
    const bool last = lane == 8;
    st_unit(wsr, pub, last ? M : o0, last ? L : o1, tag);
  }

  if (role < NTILE) {
    const int hp = role / NSL, sp = role % NSL;
    u32x4 WO[4], WD[2];
    u32x2 HG[2];
    u32 hoff[2];
    {
      const rsrc_t wor = mkrsrc(wo, NH * DH * DM * 2), wdr = mkrsrc(wd, FF * DM * 2);
      const u32 o = (u32)((hp * DH + 16 * w + 4 * r) * DM + sp * 128 + 8 * c) * 2;
#pragma unroll
      for (int i = 0; i < 2; ++i) {
        const int fr = 8 * w + 4 * i + r, f = hp * FROWS + (fr < FROWS ? fr : 0);
        hoff[i] = WS_HG + ((f >> 6) * 8 + (c & 7)) * 1024 + (c >> 3) * 512 + (f & 63) * 8;
        HG[i] = ld_gran(wsr, hoff[i]);
        WD[i] = bld<NT_>(wdr, fr < FROWS ? (u32)(f * DM + sp * 128 + 8 * c) * 2 : OOB, 0);
      }
#pragma unroll
      for (int i = 0; i < 4; ++i) WO[i] = bld<NT_>(wor, o + i * DM * 2, 0);
      SB;
    }
    if (!__all(HG[0][1] == tag && HG[1][1] == tag)) {
#pragma nounroll
      for (u32 spin = 0; spin < SPIN_CAP; ++spin) {
        asm volatile("" ::: "memory");
#pragma unroll
        for (int i = 0; i < 2; ++i) HG[i] = ld_gran(wsr, hoff[i]);
        if (__all(HG[0][1] == tag && HG[1][1] == tag)) break;
        __builtin_amdgcn_s_sleep(1);
      }
    }
    f32x4 y = zero4;
#pragma unroll
    for (int i = 0; i < 2; ++i) {
      float t = asf(HG[i][0]);
      t += dpp<XOR1>(t); t += dpp<XOR2>(t); t += dpp<HMIR>(t);
      const float o = dpp<ROR8>(t);
      const float g = rbf(c < 8 ? t : o), u = rbf(c < 8 ? o : t);
      const float hv = rbf(g * __builtin_amdgcn_rcpf(1.f + __expf(-g))) * u;
      y = vecmat(WD[i], pk_bf16(hv, hv), sel16, y);
    }
    const u32 rec = WS_H1 + (hp * NSPLIT + (c >> 1)) * REC1;
    const u32 oa = rec + (8 * w + 2 * r + (c & 1)) * 16, om = rec + 1024;
    const u32 yc = 16 * w + 2 * (lane & 7);
    u32 ypw = w * 128 + (r * 8 + c) * 4, ypr = (lane >> 3) * 128 + acc_pos(yc);
    u32 pub2 = lane < 8 ? WS_H2 + (sp * NH + hp) * 1024 + yc * 8 : OOB;
    asm volatile("" : "+v"(pub2), "+v"(ypw), "+v"(ypr));
    u32 a0, a1, m0, m1;
    {
      const u32x4 na = ld_unit(wsr, oa), nm = ld_unit(wsr, om);
      a0 = na[0]; a1 = na[2]; m0 = nm[0]; m1 = nm[2];
      const u64 need = __ballot(!(fresh(na, tag) && fresh(nm, tag)));
      if (need) poll_records(wsr, oa, om, tag, need, a0, a1, m0, m1);
    }
    float M = asf(m0);
    M = fmaxf(M, dpp<XOR2>(M)); M = fmaxf(M, dpp<ROR4>(M)); M = fmaxf(M, dpp<ROR8>(M));
    const float f = __builtin_amdgcn_exp2f(asf(m0) - M);
    float L = f * asf(m1), o0 = f * asf(a0), o1 = f * asf(a1);
    L += dpp<XOR2>(L); o0 += dpp<XOR2>(o0); o1 += dpp<XOR2>(o1);
    L += dpp<ROR4>(L); o0 += dpp<ROR4>(o0); o1 += dpp<ROR4>(o1);
    L += dpp<ROR8>(L); o0 += dpp<ROR8>(o0); o1 += dpp<ROR8>(o1);
    const float il = __builtin_amdgcn_rcpf(L);
    o0 *= il; o1 *= il;
    const u32 e0 = pk_bf16(o0, o0), e1 = pk_bf16(o1, o1), n0 = dppu<XOR1>(e0), n1 = dppu<XOR1>(e1);
    const bool odd = c & 1;
    const u32x4 b0 = diag(odd ? n0 : e0, sel16), b1 = diag(odd ? n1 : e1, sel16);
    const u32x4 b2 = diag(odd ? e0 : n0, sel16), b3 = diag(odd ? e1 : n1, sel16);
    f32x4 y2 = mm(WO[1], b1, zero4);
    y = mm(WO[0], b0, y); y2 = mm(WO[3], b3, y2); y = mm(WO[2], b2, y);
    if (c < 8) *reinterpret_cast<f32x4*>(yp_s + ypw) = y + y2;
    asm volatile("s_waitcnt lgkmcnt(0)\n\ts_barrier" ::: "memory");
    st_unit(wsr, pub2, oct_sum(yp_s[ypr]), oct_sum(yp_s[ypr + 4]), tag);
    asm volatile("s_waitcnt vmcnt(0)" ::: "memory");
  } else if (role < NTILE + NRED) {
    const int col = ((role - NTILE) * NW + w) * 4 + 2 * (lane & 1);
    const u32 off = WS_H2 + ((col >> 7) * NH + (lane >> 1)) * 1024 + (col & 127) * 8;
    const u32 xw = x32[col >> 1];
    u32 u0, u1;
    {
      const u32x4 n = ld_unit(wsr, off);
      u0 = n[0]; u1 = n[2];
      const u64 need = __ballot(!fresh(n, tag));
      asm volatile("" :: "v"(xw));
      if (need) poll_units(wsr, off, tag, need, u0, u1);
    }
    float y0 = asf(u0), y1 = asf(u1);
    y0 += dpp<XOR2>(y0); y1 += dpp<XOR2>(y1);
    y0 += dpp<ROR4>(y0); y1 += dpp<ROR4>(y1);
    y0 = oct_sum(y0); y1 = oct_sum(y1);
    if (lane < 2) out32[col >> 1] = pk_bf16(lo16(xw) + y0, hi16(xw) + y1);
    if (role == NTILE && w == 0 && lane == 0)
      __hip_atomic_store((GAS u32*)(ws + WS_EPOCH), tag, __ATOMIC_RELAXED, __HIP_MEMORY_SCOPE_AGENT);
    asm volatile("s_waitcnt vmcnt(0)" ::: "memory");
  }
}

}

extern "C" void kernel_launch(void* const* d_in, const int* in_sizes, int n_in, void* d_out, int out_size,
                              void* d_ws, size_t ws_size, hipStream_t stream) {
  (void)in_sizes; (void)n_in; (void)out_size; (void)ws_size;
  fused_decode<<<NWG, NT, 0, stream>>>((const u32*)d_in[0], (const char*)d_in[1], (const char*)d_in[2],
                                       (const char*)d_in[3], (const char*)d_in[4], (const char*)d_in[6],
                                       (const char*)d_in[7], (const char*)d_in[8], (u32*)d_out, (char*)d_ws);
}
```

```cpp
#include <hip/hip_runtime.h>
#include <stdint.h>

namespace {

typedef unsigned u32;
typedef unsigned long long u64;
typedef u32 u32x4 __attribute__((ext_vector_type(4)));
typedef u32 u32x2 __attribute__((ext_vector_type(2)));
typedef float f32x2 __attribute__((ext_vector_type(2)));
typedef float f32x4 __attribute__((ext_vector_type(4)));
typedef __bf16 bf16x2 __attribute__((ext_vector_type(2)));
typedef __bf16 bf16x8 __attribute__((ext_vector_type(8)));
typedef __amdgpu_buffer_rsrc_t rsrc_t;
#define GAS __attribute__((address_space(1)))
#define CAS __attribute__((address_space(4)))
#define SB __builtin_amdgcn_sched_barrier(0)
#define PACE() do { __builtin_amdgcn_s_waitcnt((RING - 1) | 0x0F70); SB; } while (0)

constexpr int DM = 640, NH = 32, SK = 16384, DH = 128, FF = 1728;
constexpr int NSPLIT = 8, NWG = NH * NSPLIT, NW = 8, NT = NW * 64;
constexpr int KEYS_WG = SK / NSPLIT;
constexpr u32 KV_BYTES = KEYS_WG * DH * 2, ROW = NW * 1024;
constexpr int NB = 4, NBT = KV_BYTES / ROW / NB;
constexpr int NROW = NB * NBT;
constexpr int RING = 4;
constexpr int G1 = 4, G2 = 8, G3 = 11, NPRE = 16;
constexpr int QROWS = DM / NSPLIT;
constexpr int NSL = 5;
constexpr int FROWS = FF / NH;
constexpr int FLINES = FF / 64;
constexpr u32 NTILE = NH * NSL, NRED = DM / (4 * NW);
constexpr u32 OOB = 0x7ff00000u;
constexpr int NT_ = 2, SC1 = 16;

constexpr u32 WS_EPOCH = 0;
constexpr u32 WS_TICKET = 2048;
constexpr u32 WS_QP = 4096;
constexpr u32 WS_HG = WS_QP + NWG * 1024;
constexpr u32 REC1 = 1280;
constexpr u32 WS_H1 = WS_HG + FLINES * 8 * 1024;
constexpr u32 WS_H2 = WS_H1 + NWG * REC1;
constexpr u32 WS_END = WS_H2 + NSL * NH * 1024;
constexpr u32 TICKET0 = 0x400;
constexpr u32 SPIN_CAP = 1u << 18;

constexpr float LOG2E = 1.4426950408889634f;
constexpr float SCALE = 0.08838834764831845f;

__device__ __forceinline__ float asf(u32 x) { return __builtin_bit_cast(float, x); }
__device__ __forceinline__ u32 asu(float x) { return __builtin_bit_cast(u32, x); }
__device__ __forceinline__ float lo16(u32 x) { return asf(x << 16); }
__device__ __forceinline__ float hi16(u32 x) { return asf(x & 0xffff0000u); }
__device__ __forceinline__ u32 pk_bf16(float a, float b) { bf16x2 v = {(__bf16)a, (__bf16)b}; return __builtin_bit_cast(u32, v); }
__device__ __forceinline__ float rbf(float x) { return (float)(__bf16)x; }

template <int CTRL> __device__ __forceinline__ u32 dppu(u32 x) { return __builtin_amdgcn_mov_dpp(x, CTRL, 0xf, 0xf, true); }
template <int CTRL> __device__ __forceinline__ float dpp(float x) { return asf(dppu<CTRL>(asu(x))); }
constexpr int XOR1 = 0xB1, XOR2 = 0x4E, HMIR = 0x141, ROR4 = 0x124, ROR8 = 0x128;
constexpr int BC0 = 0x00, BC1 = 0x55, BC2 = 0xAA, BC3 = 0xFF;
__device__ __forceinline__ float row_sum(float x) {
  x += dpp<XOR1>(x); x += dpp<XOR2>(x); x += dpp<ROR4>(x); x += dpp<ROR8>(x); return x;
}
__device__ __forceinline__ float fold16(float a, float b) {
  auto s = __builtin_amdgcn_permlane16_swap(asu(a), asu(b), false, false);
  return asf(s[0]) + asf(s[1]);
}
__device__ __forceinline__ float fold32(float x) {
  auto s = __builtin_amdgcn_permlane32_swap(asu(x), asu(x), false, false);
  return asf(s[0]) + asf(s[1]);
}
__device__ __forceinline__ float xrow_sum(float x) { return fold32(fold16(x, x)); }
__device__ __forceinline__ float xrow_max(float x) {
  auto s = __builtin_amdgcn_permlane16_swap(asu(x), asu(x), false, false);
  x = fmaxf(asf(s[0]), asf(s[1]));
  auto t = __builtin_amdgcn_permlane32_swap(asu(x), asu(x), false, false);
  return fmaxf(asf(t[0]), asf(t[1]));
}
__device__ __forceinline__ float oct_sum(float x) { return xrow_sum(x + dpp<ROR8>(x)); }
__device__ __forceinline__ float oct_max(float x) { return xrow_max(fmaxf(x, dpp<ROR8>(x))); }

__device__ __forceinline__ f32x4 mm(u32x4 a, u32x4 b, f32x4 acc) {
  return __builtin_amdgcn_mfma_f32_16x16x32_bf16(__builtin_bit_cast(bf16x8, a), __builtin_bit_cast(bf16x8, b), acc, 0, 0, 0);
}
__device__ __forceinline__ u32x4 diag_sel(int j) {
  const u32 half = (j & 1) ? 0xffff0000u : 0x0000ffffu;
  return u32x4{(j >> 1) == 0 ? half : 0u, (j >> 1) == 1 ? half : 0u, (j >> 1) == 2 ? half : 0u, (j >> 1) == 3 ? half : 0u};
}
__device__ __forceinline__ u32x4 diag(u32 xx, u32x4 sel) { return u32x4{xx & sel[0], xx & sel[1], xx & sel[2], xx & sel[3]}; }
__device__ __forceinline__ f32x4 vecmat(u32x4 w, u32 xx, u32x4 sel, f32x4 acc) { return mm(w, diag(xx, sel), acc); }
__device__ __forceinline__ u32 acc_pos(u32 e) { return ((e >> 5) * 8 + (e & 7)) * 4 + ((e >> 3) & 3); }

__device__ __forceinline__ rsrc_t mkrsrc(const void* p, u32 bytes) {
  return __builtin_amdgcn_make_buffer_rsrc(const_cast<void*>(p), 0, bytes, 0x00020000);
}
template <int AUX> __device__ __forceinline__ u32x4 bld(rsrc_t r, u32 voff, u32 soff) {
  return __builtin_amdgcn_raw_buffer_load_b128(r, voff, soff, AUX);
}
__device__ __forceinline__ void st_unit(rsrc_t r, u32 off, float a, float b, u32 tag) {
  __builtin_amdgcn_raw_buffer_store_b128(u32x4{asu(a), tag, asu(b), tag}, r, off, 0, SC1);
}
__device__ __forceinline__ u32x4 ld_unit(rsrc_t r, u32 off) { return bld<SC1>(r, off, 0); }
__device__ __forceinline__ u32x2 ld_gran(rsrc_t r, u32 off) { return __builtin_amdgcn_raw_buffer_load_b64(r, off, 0, SC1); }
__device__ __forceinline__ bool fresh(u32x4 u, u32 tag) { return u[1] == tag && u[3] == tag; }

#define POLL_GAP "s_sleep 5\n\t"
#define POLL_TAIL(L)                                        \
  "s_andn2_b64 %[need], %[need], vcc\n\t"                   \
  "s_cbranch_scc0 2f\n\t"                                   \
  "s_bcnt1_i32_b64 %[n], %[need]\n\t"                       \
  "s_cmp_le_u32 %[n], 8\n\t"                                \
  "s_cbranch_scc1 " L "f\n\t"                               \
  "s_sleep %[slp]\n"                                        \
  L ":\n\t"                                                 \
  "s_mov_b64 exec, %[need]\n\t"
#define POLL_LOOP_END                                       \
  "s_sub_u32 %[cnt], %[cnt], 1\n\t"                         \
  "s_cbranch_scc0 1b\n"                                     \
  "2:\n\t"                                                  \
  "s_nop 1"
#define POLL2_LD(A) "buffer_load_dwordx4 v[" #A ":" #A "+3], %[o], %[rs], 0 offen sc1\n\t"
#define POLL2_PASS(A, L)                                    \
  "s_waitcnt vmcnt(2)\n\t"                                  \
  "v_cmp_eq_u32 vcc, %[tag], v[" #A "+1]\n\t"               \
  "v_cmp_eq_u32 %[t], %[tag], v[" #A "+3]\n\t"              \
  "s_and_b64 vcc, vcc, %[t]\n\t"                            \
  "s_and_b64 vcc, vcc, %[need]\n\t"                         \
  "v_cndmask_b32 %[u0], %[u0], v[" #A "], vcc\n\t"          \
  "v_cndmask_b32 %[u1], %[u1], v[" #A "+2], vcc\n\t"        \
  POLL_TAIL(L) POLL2_LD(A) "s_mov_b64 exec, -1\n\t"
template <int SLP> __device__ __forceinline__ void poll_units(rsrc_t rs, u32 o, u32 tag, u64 need, u32& u0, u32& u1) {
  u64 t; u32 n, cnt = SPIN_CAP;
  asm volatile(
      "s_mov_b64 exec, %[need]\n\t" POLL2_LD(208) POLL_GAP POLL2_LD(212) POLL_GAP POLL2_LD(216) "s_mov_b64 exec, -1\n"
      "1:\n\t" POLL2_PASS(208, "3") POLL2_PASS(212, "4") POLL2_PASS(216, "5") POLL_LOOP_END
      : [u0] "+v"(u0), [u1] "+v"(u1), [need] "+s"(need), [cnt] "+s"(cnt), [t] "=&s"(t), [n] "=&s"(n)
      : [o] "v"(o), [rs] "s"(rs), [tag] "s"(tag), [slp] "i"(SLP)
      : "vcc", "scc", "memory", "v208", "v209", "v210", "v211", "v212", "v213", "v214", "v215", "v216", "v217", "v218", "v219");
}

__device__ __forceinline__ float quad_trace(f32x4 d) {
  float t = ((dpp<BC0>(d[0]) + dpp<BC1>(d[1])) + dpp<BC2>(d[2])) + dpp<BC3>(d[3]);
  asm("" : "+v"(t));
  return t;
}
__device__ __forceinline__ float batch_score(const f32x4 (&d)[NB], int slot) {
  const float t0 = quad_trace(d[0]), t1 = quad_trace(d[1]), t2 = quad_trace(d[2]), t3 = quad_trace(d[3]);
  const float t = (slot & 2) ? ((slot & 1) ? t3 : t2) : ((slot & 1) ? t1 : t0);
  return rbf(rbf(t) * SCALE) * LOG2E;
}
template <int I> __device__ __forceinline__ u32 quad_bc(u32 x) { return dppu<I == 0 ? BC0 : I == 1 ? BC1 : I == 2 ? BC2 : BC3>(x); }

__global__ void __launch_bounds__(NT, 2)
fused_decode(const u32* __restrict__ x32, const char* __restrict__ kc, const char* __restrict__ vc,
             const char* __restrict__ wq, const char* __restrict__ wo, const char* __restrict__ wg,
             const char* __restrict__ wu, const char* __restrict__ wd, u32* __restrict__ out32, char* ws) {
  __shared__ __attribute__((aligned(16))) float lds[5 * NW * 128 + 64 + 2 * NW + 4];
  u32* xn_s = reinterpret_cast<u32*>(lds);
  float* qp_s = lds + NW * 128;
  float* fp_s = qp_s + NW * 128;
  float* op_s = fp_s + NW * 128;
  float* yp_s = op_s + NW * 128;
  u32* q_s = reinterpret_cast<u32*>(yp_s + NW * 128);
  float* ml_s = yp_s + NW * 128 + 64;
  u32* role_s = reinterpret_cast<u32*>(ml_s + 2 * NW);

  asm volatile("" :: "s"(x32), "s"(kc), "s"(vc), "s"(wq), "s"(wo), "s"(wg), "s"(wu), "s"(wd), "s"(out32), "s"(ws));
  const int tid = threadIdx.x, lane = tid & 63, r = lane >> 4, c = lane & 15;
  const int w = __builtin_amdgcn_readfirstlane(tid >> 6);
  const int b = blockIdx.x, xs = b & 7, j = b >> 3;
  const int h = xs * 4 + (j >> 3), s = j & 7;
  const rsrc_t wsr = mkrsrc(ws, WS_END);
  const f32x4 zero4 = {0.f, 0.f, 0.f, 0.f};

  const int qrow = s * QROWS + 4 * w;
  const bool ffn_on = b < FLINES * 8;
  const int frow = xs * 80 + 8 * (w & 3);
  const size_t kv0 = (size_t)(h * NSPLIT + s) * KV_BYTES;
  const rsrc_t kr = mkrsrc(kc + kv0, KV_BYTES), vr = mkrsrc(vc + kv0, KV_BYTES);
  const u32 vo = lane * 16 + w * 1024;
  const u32 ko = (c >> 2) * 256 + (c & 3) * 64 + r * 16 + w * 1024;
  u32 row = (u32)(h * NSPLIT + s) * ROW - ROW;
  u32x4 L[2 * NROW];
  auto request = [&](int n) {
    row = (row + ROW) & (KV_BYTES - 1);
    asm volatile("" : "+s"(row));
    L[n] = n < NROW ? bld<NT_>(kr, ko, row) : bld<NT_>(vr, vo, row);
  };
  auto request_range = [&](int n0, int n1) {
#pragma unroll
    for (int n = n0; n < n1; ++n) request(n);
    SB;
  };
  auto request_paced = [&](int n0, int n1) {
#pragma unroll
    for (int n = n0; n < n1; ++n) { PACE(); request(n); SB; }
  };
  const rsrc_t xrs = mkrsrc(x32, DM * 2);
  const u32x4 xa = bld<0>(xrs, lane * 16, 0), xb = bld<0>(xrs, lane * 16 + 1024, 0);
  u32 tag = *(const CAS u32*)(ws + WS_EPOCH) + 1u;
  u32x4 WQ[3];
  {
    const rsrc_t qr = mkrsrc(wq, DM * NH * DH * 2);
    const u32 o = (u32)((qrow + r) * (NH * DH) + h * DH + 8 * c) * 2;
    WQ[0] = bld<NT_>(qr, o, 0);
    WQ[1] = bld<NT_>(qr, o + 32 * NH * DH * 2, 0);
    WQ[2] = bld<NT_>(qr, w < 4 ? o + 64 * NH * DH * 2 : OOB, 0);
    SB;
  }
  request_range(0, G1);

  {
    float xv[16], sum = 0.f;
#pragma unroll
    for (int i = 0; i < 4; ++i) { xv[2 * i] = lo16(xa[i]); xv[2 * i + 1] = hi16(xa[i]); xv[8 + 2 * i] = lo16(xb[i]); xv[9 + 2 * i] = hi16(xb[i]); }
#pragma unroll
    for (int i = 0; i < 16; ++i) sum += xv[i];
    const float mean = xrow_sum(row_sum(sum)) * (1.f / DM);
    float sq = 0.f, sq2 = 0.f;
#pragma unroll
    for (int i = 0; i < 8; ++i) { const float d = xv[i] - mean, e = xv[8 + i] - mean; sq = fmaf(d, d, sq); sq2 = fmaf(e, e, sq2); }
    const float var = xrow_sum(row_sum(lane < 16 ? sq + sq2 : sq)) * (1.f / DM);
    const float mb = rbf(mean);
    const float inv = __builtin_amdgcn_rcpf(rbf(__builtin_amdgcn_sqrtf(rbf(rbf(var) + 1e-5f))));
    u32 n[16];
#pragma unroll
    for (int i = 0; i < 16; ++i) { const float t = rbf(xv[i] - mb) * inv; n[i] = pk_bf16(t, t); }
    *reinterpret_cast<u32x4*>(xn_s + 8 * lane) = u32x4{n[0], n[1], n[2], n[3]};
    *reinterpret_cast<u32x4*>(xn_s + 8 * lane + 4) = u32x4{n[4], n[5], n[6], n[7]};
    *reinterpret_cast<u32x4*>(xn_s + 512 + 8 * lane) = u32x4{n[8], n[9], n[10], n[11]};
    *reinterpret_cast<u32x4*>(xn_s + 512 + 8 * lane + 4) = u32x4{n[12], n[13], n[14], n[15]};
  }
  SB;
  request_range(G1, G2);

  const u32x4 sel16 = diag_sel(c);
  {
    f32x4 a = zero4;
#pragma unroll
    for (int i = 0; i < 3; ++i) a = vecmat(WQ[i], xn_s[qrow + 32 * i + r], sel16, a);
    if (c < 8) *reinterpret_cast<f32x4*>(qp_s + w * 128 + (r * 8 + c) * 4) = a;
  }
  asm volatile("" : "+s"(tag));
  __syncthreads();
  if (w == 0) {
    const float* p = qp_s + acc_pos(2 * lane);
    float t0 = 0.f, t1 = 0.f;
#pragma unroll
    for (int i = 0; i < NW; ++i) { t0 += p[i * 128]; t1 += p[i * 128 + 4]; }
    st_unit(wsr, WS_QP + (h * NSPLIT + s) * 1024 + lane * 16, t0, t1, tag);
    if (b == 0 && lane == 0)
      __hip_atomic_store((GAS u32*)(ws + WS_TICKET), (tag << 12) + TICKET0, __ATOMIC_RELAXED, __HIP_MEMORY_SCOPE_AGENT);
  }
  SB;

  u32x4 WF[3];
  {
    const rsrc_t fr = mkrsrc(w < 4 ? wg : wu, ffn_on ? DM * FF * 2 : 0);
    const u32 o = (u32)((frow + (lane >> 3)) * FF + j * 64 + 8 * (lane & 7)) * 2;
    PACE(); WF[0] = bld<NT_>(fr, o, 0); SB;
    PACE(); WF[1] = bld<NT_>(fr, o + 32 * FF * 2, 0); SB;
    PACE(); WF[2] = bld<NT_>(fr, (w & 3) < 2 ? o + 64 * FF * 2 : OOB, 0);
    SB;
  }
  request_paced(G2, G3);
  const u32 qoff = WS_QP + h * NSPLIT * 1024 + lane * 16;
  u32x4 qu[NSPLIT];
  if (w == 0) {
#pragma unroll
    for (int i = 0; i < NSPLIT; ++i) qu[i] = ld_unit(wsr, qoff + i * 1024);
  }
  SB;
  request_paced(G3, NPRE);
  if (w == 0) {
    auto all_fresh = [&]() {
      bool ok = true;
#pragma unroll
      for (int i = 0; i < NSPLIT; ++i) ok = ok && fresh(qu[i], tag);
      return __all(ok);
    };
    if (!all_fresh()) {
#pragma nounroll
      for (u32 spin = 0; spin < SPIN_CAP; ++spin) {
        asm volatile("" ::: "memory");
#pragma unroll
        for (int i = 0; i < NSPLIT; ++i) qu[i] = ld_unit(wsr, qoff + i * 1024);
        if (all_fresh()) break;
      }
    }
    float t0 = 0.f, t1 = 0.f;
#pragma unroll
    for (int i = 0; i < NSPLIT; ++i) { t0 += asf(qu[i][0]); t1 += asf(qu[i][2]); }
    q_s[lane] = pk_bf16(t0, t1);
  }
  {
    const u32x4 sel8 = diag_sel(lane & 7);
    f32x4 f = zero4;
#pragma unroll
    for (int i = 0; i < 3; ++i) f = vecmat(WF[i], xn_s[frow + 32 * i + (lane >> 3)], sel8, f);
    if ((c >> 3) == (r >> 1)) *reinterpret_cast<f32x4*>(fp_s + w * 128 + (r * 8 + (c & 7)) * 4) = f;
  }
  __syncthreads();
  const u32x4 q = *reinterpret_cast<const u32x4*>(q_s + 16 * (c & 3) + 4 * r);
  if (w == NW - 1 && ffn_on) {
    const float* p = fp_s + (lane >> 5) * 512 + acc_pos(2 * (lane & 31));
    float g0 = 0.f, g1 = 0.f;
#pragma unroll
    for (int i = 0; i < 8; ++i) { g0 += p[i * 64]; g1 += p[i * 64 + 4]; }
    st_unit(wsr, WS_HG + b * 1024 + lane * 16, g0, g1, tag);
  }

  auto refill = [&](int n) {
    const int nx = n < RING ? NPRE + n : n >= NPRE ? n + RING : 2 * NROW;
    if (nx < 2 * NROW) { SB; request(nx); SB; }
  };
  float sv[NBT];
#pragma unroll
  for (int t = 0; t < NBT; ++t) {
    f32x4 d[NB];
#pragma unroll
    for (int i = 0; i < NB; ++i) { d[i] = mm(L[NB * t + i], q, zero4); refill(NB * t + i); }
    sv[t] = batch_score(d, c & 3);
    asm volatile("" : "+v"(sv[t]));
  }
  float mx = sv[0];
#pragma unroll
  for (int t = 1; t < NBT; ++t) mx = fmaxf(mx, sv[t]);
  mx = fmaxf(mx, dpp<XOR1>(mx));
  mx = xrow_max(fmaxf(mx, dpp<XOR2>(mx)));
  float pw[NBT], ls = 0.f;
#pragma unroll
  for (int t = 0; t < NBT; ++t) { pw[t] = __builtin_amdgcn_exp2f(sv[t] - mx); ls += pw[t]; }
  ls += dpp<XOR1>(ls);
  ls = xrow_sum(ls + dpp<XOR2>(ls));
  if (lane == 0) *reinterpret_cast<f32x2*>(ml_s + 2 * w) = f32x2{mx, ls};
  asm volatile("s_waitcnt lgkmcnt(0)\n\ts_barrier" ::: "memory");
  u32 pp[NBT];
  {
    const f32x2 ml = *reinterpret_cast<const f32x2*>(ml_s + 2 * (lane >> 3));
    const float M = oct_max(ml[0]), L = oct_sum(__builtin_amdgcn_exp2f(ml[0] - M) * ml[1]);
    st_unit(wsr, (w == 0 && lane == 0) ? WS_H1 + (h * NSPLIT + s) * REC1 + 1024 : OOB, M, L, tag);
    const float f = __builtin_amdgcn_exp2f(mx - M);
#pragma unroll
    for (int t = 0; t < NBT; ++t) { const float p = pw[t] * f; pp[t] = pk_bf16(p, p); }
  }
  const u32 toff = (w == 1 && lane == 0) ? WS_TICKET : OOB;
  u32 ticket = 0;
  f32x4 acc = zero4;
#pragma unroll
  for (int t = 0; t < NBT - 1; ++t) {
    if (t == NBT - 2) { SB; ticket = __builtin_amdgcn_raw_ptr_buffer_atomic_add_i32(1, wsr, toff, 0, 0); SB; }
    const int n = NROW + NB * t;
    acc = vecmat(L[n], quad_bc<0>(pp[t]), sel16, acc); refill(n);
    acc = vecmat(L[n + 1], quad_bc<1>(pp[t]), sel16, acc); refill(n + 1);
    acc = vecmat(L[n + 2], quad_bc<2>(pp[t]), sel16, acc); refill(n + 2);
    acc = vecmat(L[n + 3], quad_bc<3>(pp[t]), sel16, acc); refill(n + 3);
  }
  u32 opr = (lane >> 3) * DH + acc_pos(16 * w + 2 * (lane & 7)), opw = w * DH + (r * 8 + c) * 4;
  u32 pub = lane < 8 ? WS_H1 + (h * NSPLIT + s) * REC1 + (8 * w + lane) * 16 : OOB;
  asm volatile("" : "+v"(pub), "+v"(opr), "+v"(opw));
  SB;
  {
    constexpr int n = 2 * NROW - NB;
    acc = vecmat(L[n], quad_bc<0>(pp[NBT - 1]), sel16, acc); acc = vecmat(L[n + 1], quad_bc<1>(pp[NBT - 1]), sel16, acc);
    acc = vecmat(L[n + 2], quad_bc<2>(pp[NBT - 1]), sel16, acc); acc = vecmat(L[n + 3], quad_bc<3>(pp[NBT - 1]), sel16, acc);
  }
  if (c < 8) *reinterpret_cast<f32x4*>(op_s + opw) = acc;
  if (w == 1) {
    auto valid = [&](u32 v) { return (v >> 12) == (tag & 0xfffffu) && (v & 0xfffu) >= TICKET0; };
    u32 tk = __builtin_amdgcn_readfirstlane(ticket);
    if (!valid(tk)) {
#pragma nounroll
      for (u32 spin = 0; spin < SPIN_CAP; ++spin) {
        __builtin_amdgcn_s_sleep(8);
        asm volatile("" ::: "memory");
        if (valid(__builtin_amdgcn_readfirstlane(__builtin_amdgcn_raw_buffer_load_b32(wsr, toff, 0, SC1)))) break;
      }
      tk = __builtin_amdgcn_readfirstlane(__builtin_amdgcn_raw_ptr_buffer_atomic_add_i32(1, wsr, toff, 0, 0));
    }
    if (lane == 0) *role_s = (tk & 0xfffu) - TICKET0;
  }
  __syncthreads();
  const u32 role = __builtin_amdgcn_readfirstlane(*role_s);
  st_unit(wsr, pub, oct_sum(op_s[opr]), oct_sum(op_s[opr + 4]), tag);
  if (role < NTILE) {
    const int hp = role / NSL, sp = role % NSL;
    const u32 rec = WS_H1 + (hp * NSPLIT + (c >> 1)) * REC1;
    const u32 oa = rec + (8 * w + 2 * r + (c & 1)) * 16, om = rec + 1024;
    const u32x4 nm = ld_unit(wsr, om);
    u32x4 WO[4], WD[2], na;
    u32x2 HG[2];
    u32 hoff[2];
    {
      const rsrc_t wor = mkrsrc(wo, NH * DH * DM * 2), wdr = mkrsrc(wd, FF * DM * 2);
      const u32 o = (u32)((hp * DH + 16 * w + 4 * r) * DM + sp * 128 + 8 * c) * 2;
#pragma unroll
      for (int i = 0; i < 2; ++i) {
        const int fr = 8 * w + 4 * i + r, f = hp * FROWS + (fr < FROWS ? fr : 0);
        hoff[i] = WS_HG + ((f >> 6) * 8 + (c & 7)) * 1024 + (c >> 3) * 512 + (f & 63) * 8;
        HG[i] = ld_gran(wsr, hoff[i]);
        WD[i] = bld<NT_>(wdr, fr < FROWS ? (u32)(f * DM + sp * 128 + 8 * c) * 2 : OOB, 0);
      }
#pragma unroll
      for (int i = 0; i < 4; ++i) WO[i] = bld<NT_>(wor, o + i * DM * 2, 0);
      SB;
      na = ld_unit(wsr, oa);
      SB;
    }
    if (!__all(HG[0][1] == tag && HG[1][1] == tag)) {
#pragma nounroll
      for (u32 spin = 0; spin < SPIN_CAP; ++spin) {
        asm volatile("" ::: "memory");
#pragma unroll
        for (int i = 0; i < 2; ++i) HG[i] = ld_gran(wsr, hoff[i]);
        if (__all(HG[0][1] == tag && HG[1][1] == tag)) break;
        __builtin_amdgcn_s_sleep(1);
      }
    }
    f32x4 y = zero4;
#pragma unroll
    for (int i = 0; i < 2; ++i) {
      float t = asf(HG[i][0]);
      t += dpp<XOR1>(t); t += dpp<XOR2>(t); t += dpp<HMIR>(t);
      const float o = dpp<ROR8>(t);
      const float g = rbf(c < 8 ? t : o), u = rbf(c < 8 ? o : t);
      const float hv = rbf(g * __builtin_amdgcn_rcpf(1.f + __expf(-g))) * u;
      y = vecmat(WD[i], pk_bf16(hv, hv), sel16, y);
    }
    const u32 yc = 16 * w + 2 * (lane & 7);
    u32 ypw = w * 128 + (r * 8 + c) * 4, ypr = (lane >> 3) * 128 + acc_pos(yc);
    u32 pub2 = lane < 8 ? WS_H2 + (sp * NH + hp) * 1024 + yc * 8 : OOB;
    asm volatile("" : "+v"(pub2), "+v"(ypw), "+v"(ypr));
    u32 m0, m1;
    {
      m0 = nm[0]; m1 = nm[2];
      const u64 need = __ballot(!fresh(nm, tag));
      if (need) {
        asm volatile("" : "+v"(na));
        poll_units<2>(wsr, om, tag, need, m0, m1);
        asm volatile("s_waitcnt vmcnt(0)" ::: "memory");
      }
    }
    float M = asf(m0);
    M = fmaxf(M, dpp<XOR2>(M)); M = fmaxf(M, dpp<ROR4>(M)); M = fmaxf(M, dpp<ROR8>(M));
    float f = __builtin_amdgcn_exp2f(asf(m0) - M), L = f * asf(m1);
    L += dpp<XOR2>(L); L += dpp<ROR4>(L); L += dpp<ROR8>(L);
    f *= __builtin_amdgcn_rcpf(L);
    asm volatile("" : "+v"(f));
    u32 a0, a1;
    {
      a0 = na[0]; a1 = na[2];
      const u64 need = __ballot(!fresh(na, tag));
      if (need) poll_units<2>(wsr, oa, tag, need, a0, a1);
    }
    float o0 = f * asf(a0), o1 = f * asf(a1);
    o0 += dpp<XOR2>(o0); o1 += dpp<XOR2>(o1);
    o0 += dpp<ROR4>(o0); o1 += dpp<ROR4>(o1);
    o0 += dpp<ROR8>(o0); o1 += dpp<ROR8>(o1);
    const u32 e0 = pk_bf16(o0, o0), e1 = pk_bf16(o1, o1), n0 = dppu<XOR1>(e0), n1 = dppu<XOR1>(e1);
    const bool odd = c & 1;
    const u32x4 b0 = diag(odd ? n0 : e0, sel16), b1 = diag(odd ? n1 : e1, sel16);
    const u32x4 b2 = diag(odd ? e0 : n0, sel16), b3 = diag(odd ? e1 : n1, sel16);
    f32x4 y2 = mm(WO[1], b1, zero4);
    y = mm(WO[0], b0, y); y2 = mm(WO[3], b3, y2); y = mm(WO[2], b2, y);
    if (c < 8) *reinterpret_cast<f32x4*>(yp_s + ypw) = y + y2;
    asm volatile("s_waitcnt lgkmcnt(0)\n\ts_barrier" ::: "memory");
    st_unit(wsr, pub2, oct_sum(yp_s[ypr]), oct_sum(yp_s[ypr + 4]), tag);
    asm volatile("s_waitcnt vmcnt(0)" ::: "memory");
  } else if (role < NTILE + NRED) {
    const int col = ((role - NTILE) * NW + w) * 4 + 2 * (lane & 1);
    const u32 off = WS_H2 + ((col >> 7) * NH + (lane >> 1)) * 1024 + (col & 127) * 8;
    const u32 xw = x32[col >> 1];
    u32 u0, u1;
    {
      const u32x4 n = ld_unit(wsr, off);
      u0 = n[0]; u1 = n[2];
      const u64 need = __ballot(!fresh(n, tag));
      asm volatile("" :: "v"(xw));
      if (need) poll_units<8>(wsr, off, tag, need, u0, u1);
    }
    float y0 = asf(u0), y1 = asf(u1);
    y0 += dpp<XOR2>(y0); y1 += dpp<XOR2>(y1);
    y0 += dpp<ROR4>(y0); y1 += dpp<ROR4>(y1);
    y0 = oct_sum(y0); y1 = oct_sum(y1);
    if (lane < 2) out32[col >> 1] = pk_bf16(lo16(xw) + y0, hi16(xw) + y1);
    if (role == NTILE && w == 0 && lane == 0)
      __hip_atomic_store((GAS u32*)(ws + WS_EPOCH), tag, __ATOMIC_RELAXED, __HIP_MEMORY_SCOPE_AGENT);
    asm volatile("s_waitcnt vmcnt(0)" ::: "memory");
  }
}

}

extern "C" void kernel_launch(void* const* d_in, const int* in_sizes, int n_in, void* d_out, int out_size,
                              void* d_ws, size_t ws_size, hipStream_t stream) {
  (void)in_sizes; (void)n_in; (void)out_size; (void)ws_size;
  fused_decode<<<NWG, NT, 0, stream>>>((const u32*)d_in[0], (const char*)d_in[1], (const char*)d_in[2],
                                       (const char*)d_in[3], (const char*)d_in[4], (const char*)d_in[6],
                                       (const char*)d_in[7], (const char*)d_in[8], (u32*)d_out, (char*)d_ws);
}
```

```cpp
#include <hip/hip_runtime.h>
#include <stdint.h>

namespace {

typedef unsigned u32;
typedef unsigned long long u64;
typedef u32 u32x4 __attribute__((ext_vector_type(4)));
typedef u32 u32x2 __attribute__((ext_vector_type(2)));
typedef float f32x2 __attribute__((ext_vector_type(2)));
typedef float f32x4 __attribute__((ext_vector_type(4)));
typedef __bf16 bf16x2 __attribute__((ext_vector_type(2)));
typedef __bf16 bf16x8 __attribute__((ext_vector_type(8)));
typedef __amdgpu_buffer_rsrc_t rsrc_t;
#define GAS __attribute__((address_space(1)))
#define CAS __attribute__((address_space(4)))
#define SB __builtin_amdgcn_sched_barrier(0)
#define PACE() do { __builtin_amdgcn_s_waitcnt((RING - 1) | 0x0F70); SB; } while (0)

constexpr int DM = 640, NH = 32, SK = 16384, DH = 128, FF = 1728;
constexpr int NSPLIT = 8, NWG = NH * NSPLIT, NW = 8, NT = NW * 64;
constexpr int KEYS_WG = SK / NSPLIT;
constexpr u32 KV_BYTES = KEYS_WG * DH * 2, ROW = NW * 1024;
constexpr int NB = 4, NBT = KV_BYTES / ROW / NB;
constexpr int NROW = NB * NBT;
constexpr int RING = 4;
constexpr int G1 = 4, G2 = 8, G3 = 11, NPRE = 16;
constexpr int QROWS = DM / NSPLIT;
constexpr int NSL = 5;
constexpr int FROWS = FF / NH;
constexpr int FLINES = FF / 64;
constexpr u32 NTILE = NH * NSL, NRED = DM / (4 * NW);
constexpr u32 OOB = 0x7ff00000u;
constexpr int NT_ = 2, SC1 = 16;

constexpr u32 WS_EPOCH = 0;
constexpr u32 WS_TICKET = 2048;
constexpr u32 WS_QP = 4096;
constexpr u32 WS_HG = WS_QP + NWG * 1024;
constexpr u32 REC1 = 1280;
constexpr u32 WS_H1 = WS_HG + FLINES * 8 * 1024;
constexpr u32 WS_H2 = WS_H1 + NWG * REC1;
constexpr u32 WS_END = WS_H2 + NSL * NH * 1024;
constexpr u32 TICKET0 = 0x400;
constexpr u32 SPIN_CAP = 1u << 18;

constexpr float LOG2E = 1.4426950408889634f;
constexpr float SCALE = 0.08838834764831845f;

__device__ __forceinline__ float asf(u32 x) { return __builtin_bit_cast(float, x); }
__device__ __forceinline__ u32 asu(float x) { return __builtin_bit_cast(u32, x); }
__device__ __forceinline__ float lo16(u32 x) { return asf(x << 16); }
__device__ __forceinline__ float hi16(u32 x) { return asf(x & 0xffff0000u); }
__device__ __forceinline__ u32 pk_bf16(float a, float b) { bf16x2 v = {(__bf16)a, (__bf16)b}; return __builtin_bit_cast(u32, v); }
__device__ __forceinline__ float rbf(float x) { return (float)(__bf16)x; }

template <int CTRL> __device__ __forceinline__ u32 dppu(u32 x) { return __builtin_amdgcn_mov_dpp(x, CTRL, 0xf, 0xf, true); }
template <int CTRL> __device__ __forceinline__ float dpp(float x) { return asf(dppu<CTRL>(asu(x))); }
constexpr int XOR1 = 0xB1, XOR2 = 0x4E, HMIR = 0x141, ROR4 = 0x124, ROR8 = 0x128;
constexpr int BC0 = 0x00, BC1 = 0x55, BC2 = 0xAA, BC3 = 0xFF;
__device__ __forceinline__ float row_sum(float x) {
  x += dpp<XOR1>(x); x += dpp<XOR2>(x); x += dpp<ROR4>(x); x += dpp<ROR8>(x); return x;
}
__device__ __forceinline__ float fold16(float a, float b) {
  auto s = __builtin_amdgcn_permlane16_swap(asu(a), asu(b), false, false);
  return asf(s[0]) + asf(s[1]);
}
__device__ __forceinline__ float fold32(float x) {
  auto s = __builtin_amdgcn_permlane32_swap(asu(x), asu(x), false, false);
  return asf(s[0]) + asf(s[1]);
}
__device__ __forceinline__ float xrow_sum(float x) { return fold32(fold16(x, x)); }
__device__ __forceinline__ float xrow_max(float x) {
  auto s = __builtin_amdgcn_permlane16_swap(asu(x), asu(x), false, false);
  x = fmaxf(asf(s[0]), asf(s[1]));
  auto t = __builtin_amdgcn_permlane32_swap(asu(x), asu(x), false, false);
  return fmaxf(asf(t[0]), asf(t[1]));
}
__device__ __forceinline__ float oct_sum(float x) { return xrow_sum(x + dpp<ROR8>(x)); }
__device__ __forceinline__ float oct_max(float x) { return xrow_max(fmaxf(x, dpp<ROR8>(x))); }

__device__ __forceinline__ f32x4 mm(u32x4 a, u32x4 b, f32x4 acc) {
  return __builtin_amdgcn_mfma_f32_16x16x32_bf16(__builtin_bit_cast(bf16x8, a), __builtin_bit_cast(bf16x8, b), acc, 0, 0, 0);
}
__device__ __forceinline__ u32x4 diag_sel(int j) {
  const u32 half = (j & 1) ? 0xffff0000u : 0x0000ffffu;
  return u32x4{(j >> 1) == 0 ? half : 0u, (j >> 1) == 1 ? half : 0u, (j >> 1) == 2 ? half : 0u, (j >> 1) == 3 ? half : 0u};
}
__device__ __forceinline__ u32x4 diag(u32 xx, u32x4 sel) { return u32x4{xx & sel[0], xx & sel[1], xx & sel[2], xx & sel[3]}; }
__device__ __forceinline__ f32x4 vecmat(u32x4 w, u32 xx, u32x4 sel, f32x4 acc) { return mm(w, diag(xx, sel), acc); }
__device__ __forceinline__ u32 acc_pos(u32 e) { return ((e >> 5) * 8 + (e & 7)) * 4 + ((e >> 3) & 3); }

__device__ __forceinline__ rsrc_t mkrsrc(const void* p, u32 bytes) {
  return __builtin_amdgcn_make_buffer_rsrc(const_cast<void*>(p), 0, bytes, 0x00020000);
}
template <int AUX> __device__ __forceinline__ u32x4 bld(rsrc_t r, u32 voff, u32 soff) {
  return __builtin_amdgcn_raw_buffer_load_b128(r, voff, soff, AUX);
}
template <int AUX = SC1> __device__ __forceinline__ void st_unit(rsrc_t r, u32 off, float a, float b, u32 tag) {
  __builtin_amdgcn_raw_buffer_store_b128(u32x4{asu(a), tag, asu(b), tag}, r, off, 0, AUX);
}
__device__ __forceinline__ u32x4 ld_unit(rsrc_t r, u32 off) { return bld<SC1>(r, off, 0); }
__device__ __forceinline__ u32x2 ld_gran(rsrc_t r, u32 off) { return __builtin_amdgcn_raw_buffer_load_b64(r, off, 0, SC1); }
__device__ __forceinline__ bool fresh(u32x4 u, u32 tag) { return u[1] == tag && u[3] == tag; }

#define POLL_GAP "s_sleep 5\n\t"
#define POLL_TAIL(L)                                        \
  "s_andn2_b64 %[need], %[need], vcc\n\t"                   \
  "s_cbranch_scc0 2f\n\t"                                   \
  "s_bcnt1_i32_b64 %[n], %[need]\n\t"                       \
  "s_cmp_le_u32 %[n], 8\n\t"                                \
  "s_cbranch_scc1 " L "f\n\t"                               \
  "s_sleep %[slp]\n"                                        \
  L ":\n\t"                                                 \
  "s_mov_b64 exec, %[need]\n\t"
#define POLL_LOOP_END                                       \
  "s_sub_u32 %[cnt], %[cnt], 1\n\t"                         \
  "s_cbranch_scc0 1b\n"                                     \
  "2:\n\t"                                                  \
  "s_nop 1"
#define POLL2_LD(A) "buffer_load_dwordx4 v[" #A ":" #A "+3], %[o], %[rs], 0 offen sc1\n\t"
#define POLL2_PASS(A, L)                                    \
  "s_waitcnt vmcnt(2)\n\t"                                  \
  "v_cmp_eq_u32 vcc, %[tag], v[" #A "+1]\n\t"               \
  "v_cmp_eq_u32 %[t], %[tag], v[" #A "+3]\n\t"              \
  "s_and_b64 vcc, vcc, %[t]\n\t"                            \
  "s_and_b64 vcc, vcc, %[need]\n\t"                         \
  "v_cndmask_b32 %[u0], %[u0], v[" #A "], vcc\n\t"          \
  "v_cndmask_b32 %[u1], %[u1], v[" #A "+2], vcc\n\t"        \
  POLL_TAIL(L) POLL2_LD(A) "s_mov_b64 exec, -1\n\t"
template <int SLP> __device__ __forceinline__ void poll_units(rsrc_t rs, u32 o, u32 tag, u64 need, u32& u0, u32& u1) {
  u64 t; u32 n, cnt = SPIN_CAP;
  asm volatile(
      "s_mov_b64 exec, %[need]\n\t" POLL2_LD(208) POLL_GAP POLL2_LD(212) POLL_GAP POLL2_LD(216) "s_mov_b64 exec, -1\n"
      "1:\n\t" POLL2_PASS(208, "3") POLL2_PASS(212, "4") POLL2_PASS(216, "5") POLL_LOOP_END
      : [u0] "+v"(u0), [u1] "+v"(u1), [need] "+s"(need), [cnt] "+s"(cnt), [t] "=&s"(t), [n] "=&s"(n)
      : [o] "v"(o), [rs] "s"(rs), [tag] "s"(tag), [slp] "i"(SLP)
      : "vcc", "scc", "memory", "v208", "v209", "v210", "v211", "v212", "v213", "v214", "v215", "v216", "v217", "v218", "v219");
}

__device__ __forceinline__ float quad_trace(f32x4 d) {
  float t = ((dpp<BC0>(d[0]) + dpp<BC1>(d[1])) + dpp<BC2>(d[2])) + dpp<BC3>(d[3]);
  asm("" : "+v"(t));
  return t;
}
__device__ __forceinline__ float batch_score(const f32x4 (&d)[NB], int slot) {
  const float t0 = quad_trace(d[0]), t1 = quad_trace(d[1]), t2 = quad_trace(d[2]), t3 = quad_trace(d[3]);
  const float t = (slot & 2) ? ((slot & 1) ? t3 : t2) : ((slot & 1) ? t1 : t0);
  return rbf(rbf(t) * SCALE) * LOG2E;
}
template <int I> __device__ __forceinline__ u32 quad_bc(u32 x) { return dppu<I == 0 ? BC0 : I == 1 ? BC1 : I == 2 ? BC2 : BC3>(x); }

__global__ void __launch_bounds__(NT, 2)
fused_decode(const u32* __restrict__ x32, const char* __restrict__ kc, const char* __restrict__ vc,
             const char* __restrict__ wq, const char* __restrict__ wo, const char* __restrict__ wg,
             const char* __restrict__ wu, const char* __restrict__ wd, u32* __restrict__ out32, char* ws) {
  __shared__ __attribute__((aligned(16))) float lds[5 * NW * 128 + 64 + 2 * NW + 4];
  u32* xn_s = reinterpret_cast<u32*>(lds);
  float* qp_s = lds + NW * 128;
  float* fp_s = qp_s + NW * 128;
  float* op_s = fp_s + NW * 128;
  float* yp_s = op_s + NW * 128;
  u32* q_s = reinterpret_cast<u32*>(yp_s + NW * 128);
  float* ml_s = yp_s + NW * 128 + 64;
  u32* role_s = reinterpret_cast<u32*>(ml_s + 2 * NW);

  asm volatile("" :: "s"(x32), "s"(kc), "s"(vc), "s"(wq), "s"(wo), "s"(wg), "s"(wu), "s"(wd), "s"(out32), "s"(ws));
  const int tid = threadIdx.x, lane = tid & 63, r = lane >> 4, c = lane & 15;
  const int w = __builtin_amdgcn_readfirstlane(tid >> 6);
  const int b = blockIdx.x, xs = b & 7, j = b >> 3;
  const int h = xs * 4 + (j >> 3), s = j & 7;
  const rsrc_t wsr = mkrsrc(ws, WS_END);
  const f32x4 zero4 = {0.f, 0.f, 0.f, 0.f};

  const int qrow = s * QROWS + 4 * w;
  const bool ffn_on = b < FLINES * 8;
  const int frow = xs * 80 + 8 * (w & 3);
  const size_t kv0 = (size_t)(h * NSPLIT + s) * KV_BYTES;
  const rsrc_t kr = mkrsrc(kc + kv0, KV_BYTES), vr = mkrsrc(vc + kv0, KV_BYTES);
  const u32 vo = lane * 16 + w * 1024;
  const u32 ko = (c >> 2) * 256 + (c & 3) * 64 + r * 16 + w * 1024;
  u32 row = (u32)(h * NSPLIT + s) * ROW - ROW;
  u32x4 L[2 * NROW];
  auto request = [&](int n) {
    row = (row + ROW) & (KV_BYTES - 1);
    asm volatile("" : "+s"(row));
    L[n] = n < NROW ? bld<NT_>(kr, ko, row) : bld<NT_>(vr, vo, row);
  };
  auto request_range = [&](int n0, int n1) {
#pragma unroll
    for (int n = n0; n < n1; ++n) request(n);
    SB;
  };
  auto request_paced = [&](int n0, int n1) {
#pragma unroll
    for (int n = n0; n < n1; ++n) { PACE(); request(n); SB; }
  };
  const rsrc_t xrs = mkrsrc(x32, DM * 2);
  const u32x4 xa = bld<0>(xrs, lane * 16, 0), xb = bld<0>(xrs, lane * 16 + 1024, 0);
  u32 tag = *(const CAS u32*)(ws + WS_EPOCH) + 1u;
  u32x4 WQ[3];
  {
    const rsrc_t qr = mkrsrc(wq, DM * NH * DH * 2);
    const u32 o = (u32)((qrow + r) * (NH * DH) + h * DH + 8 * c) * 2;
    WQ[0] = bld<NT_>(qr, o, 0);
    WQ[1] = bld<NT_>(qr, o + 32 * NH * DH * 2, 0);
    WQ[2] = bld<NT_>(qr, w < 4 ? o + 64 * NH * DH * 2 : OOB, 0);
    SB;
  }
  request_range(0, G1);

  {
    float xv[16], sum = 0.f;
#pragma unroll
    for (int i = 0; i < 4; ++i) { xv[2 * i] = lo16(xa[i]); xv[2 * i + 1] = hi16(xa[i]); xv[8 + 2 * i] = lo16(xb[i]); xv[9 + 2 * i] = hi16(xb[i]); }
#pragma unroll
    for (int i = 0; i < 16; ++i) sum += xv[i];
    const float mean = xrow_sum(row_sum(sum)) * (1.f / DM);
    float sq = 0.f, sq2 = 0.f;
#pragma unroll
    for (int i = 0; i < 8; ++i) { const float d = xv[i] - mean, e = xv[8 + i] - mean; sq = fmaf(d, d, sq); sq2 = fmaf(e, e, sq2); }
    const float var = xrow_sum(row_sum(lane < 16 ? sq + sq2 : sq)) * (1.f / DM);
    const float mb = rbf(mean);
    const float inv = __builtin_amdgcn_rcpf(rbf(__builtin_amdgcn_sqrtf(rbf(rbf(var) + 1e-5f))));
    u32 n[16];
#pragma unroll
    for (int i = 0; i < 16; ++i) { const float t = rbf(xv[i] - mb) * inv; n[i] = pk_bf16(t, t); }
    *reinterpret_cast<u32x4*>(xn_s + 8 * lane) = u32x4{n[0], n[1], n[2], n[3]};
    *reinterpret_cast<u32x4*>(xn_s + 8 * lane + 4) = u32x4{n[4], n[5], n[6], n[7]};
    *reinterpret_cast<u32x4*>(xn_s + 512 + 8 * lane) = u32x4{n[8], n[9], n[10], n[11]};
    *reinterpret_cast<u32x4*>(xn_s + 512 + 8 * lane + 4) = u32x4{n[12], n[13], n[14], n[15]};
  }
  SB;
  request_range(G1, G2);

  const u32x4 sel16 = diag_sel(c);
  {
    f32x4 a = zero4;
#pragma unroll
    for (int i = 0; i < 3; ++i) a = vecmat(WQ[i], xn_s[qrow + 32 * i + r], sel16, a);
    if (c < 8) *reinterpret_cast<f32x4*>(qp_s + w * 128 + (r * 8 + c) * 4) = a;
  }
  asm volatile("" : "+s"(tag));
  __syncthreads();
  if (w == 0) {
    const float* p = qp_s + acc_pos(2 * lane);
    float t0 = 0.f, t1 = 0.f;
#pragma unroll
    for (int i = 0; i < NW; ++i) { t0 += p[i * 128]; t1 += p[i * 128 + 4]; }
    st_unit(wsr, WS_QP + (h * NSPLIT + s) * 1024 + lane * 16, t0, t1, tag);
    if (b == 0 && lane == 0)
      __hip_atomic_store((GAS u32*)(ws + WS_TICKET), (tag << 12) + TICKET0, __ATOMIC_RELAXED, __HIP_MEMORY_SCOPE_AGENT);
  }
  SB;

  u32x4 WF[3];
  {
    const rsrc_t fr = mkrsrc(w < 4 ? wg : wu, ffn_on ? DM * FF * 2 : 0);
    const u32 o = (u32)((frow + (lane >> 3)) * FF + j * 64 + 8 * (lane & 7)) * 2;
    PACE(); WF[0] = bld<NT_>(fr, o, 0); SB;
    PACE(); WF[1] = bld<NT_>(fr, o + 32 * FF * 2, 0); SB;
    PACE(); WF[2] = bld<NT_>(fr, (w & 3) < 2 ? o + 64 * FF * 2 : OOB, 0);
    SB;
  }
  request_paced(G2, G3);
  const u32 qoff = WS_QP + h * NSPLIT * 1024 + lane * 16;
  u32x4 qu[NSPLIT];
  if (w == 0) {
#pragma unroll
    for (int i = 0; i < NSPLIT; ++i) qu[i] = ld_unit(wsr, qoff + i * 1024);
  }
  SB;
  request_paced(G3, NPRE);
  if (w == 0) {
    auto all_fresh = [&]() {
      bool ok = true;
#pragma unroll
      for (int i = 0; i < NSPLIT; ++i) ok = ok && fresh(qu[i], tag);
      return __all(ok);
    };
    if (!all_fresh()) {
#pragma nounroll
      for (u32 spin = 0; spin < SPIN_CAP; ++spin) {
        asm volatile("" ::: "memory");
#pragma unroll
        for (int i = 0; i < NSPLIT; ++i) qu[i] = ld_unit(wsr, qoff + i * 1024);
        if (all_fresh()) break;
      }
    }
    float t0 = 0.f, t1 = 0.f;
#pragma unroll
    for (int i = 0; i < NSPLIT; ++i) { t0 += asf(qu[i][0]); t1 += asf(qu[i][2]); }
    q_s[lane] = pk_bf16(t0, t1);
  }
  {
    const u32x4 sel8 = diag_sel(lane & 7);
    f32x4 f = zero4;
#pragma unroll
    for (int i = 0; i < 3; ++i) f = vecmat(WF[i], xn_s[frow + 32 * i + (lane >> 3)], sel8, f);
    if ((c >> 3) == (r >> 1)) *reinterpret_cast<f32x4*>(fp_s + w * 128 + (r * 8 + (c & 7)) * 4) = f;
  }
  __syncthreads();
  const u32x4 q = *reinterpret_cast<const u32x4*>(q_s + 16 * (c & 3) + 4 * r);
  if (w == NW - 1 && ffn_on) {
    const float* p = fp_s + (lane >> 5) * 512 + acc_pos(2 * (lane & 31));
    float g0 = 0.f, g1 = 0.f;
#pragma unroll
    for (int i = 0; i < 8; ++i) { g0 += p[i * 64]; g1 += p[i * 64 + 4]; }
    st_unit<SC1 | NT_>(wsr, WS_HG + b * 1024 + lane * 16, g0, g1, tag);
  }

  auto refill = [&](int n) {
    const int nx = n < RING ? NPRE + n : n >= NPRE ? n + RING : 2 * NROW;
    if (nx < 2 * NROW) { SB; request(nx); SB; }
  };
  float sv[NBT];
#pragma unroll
  for (int t = 0; t < NBT; ++t) {
    f32x4 d[NB];
#pragma unroll
    for (int i = 0; i < NB; ++i) { d[i] = mm(L[NB * t + i], q, zero4); refill(NB * t + i); }
    sv[t] = batch_score(d, c & 3);
    asm volatile("" : "+v"(sv[t]));
  }
  float mx = sv[0];
#pragma unroll
  for (int t = 1; t < NBT; ++t) mx = fmaxf(mx, sv[t]);
  mx = fmaxf(mx, dpp<XOR1>(mx));
  mx = xrow_max(fmaxf(mx, dpp<XOR2>(mx)));
  float pw[NBT], ls = 0.f;
#pragma unroll
  for (int t = 0; t < NBT; ++t) { pw[t] = __builtin_amdgcn_exp2f(sv[t] - mx); ls += pw[t]; }
  ls += dpp<XOR1>(ls);
  ls = xrow_sum(ls + dpp<XOR2>(ls));
  if (lane == 0) *reinterpret_cast<f32x2*>(ml_s + 2 * w) = f32x2{mx, ls};
  asm volatile("s_waitcnt lgkmcnt(0)\n\ts_barrier" ::: "memory");
  u32 pp[NBT];
  {
    const f32x2 ml = *reinterpret_cast<const f32x2*>(ml_s + 2 * (lane >> 3));
    const float M = oct_max(ml[0]), L = oct_sum(__builtin_amdgcn_exp2f(ml[0] - M) * ml[1]);
    st_unit<SC1 | NT_>(wsr, (w == 0 && lane == 0) ? WS_H1 + (h * NSPLIT + s) * REC1 + 1024 : OOB, M, L, tag);
    const float f = __builtin_amdgcn_exp2f(mx - M);
#pragma unroll
    for (int t = 0; t < NBT; ++t) { const float p = pw[t] * f; pp[t] = pk_bf16(p, p); }
  }
  const u32 toff = (w == 1 && lane == 0) ? WS_TICKET : OOB;
  u32 ticket = 0;
  f32x4 acc = zero4;
#pragma unroll
  for (int t = 0; t < NBT - 1; ++t) {
    if (t == NBT - 2) { SB; ticket = __builtin_amdgcn_raw_ptr_buffer_atomic_add_i32(1, wsr, toff, 0, 0); SB; }
    const int n = NROW + NB * t;
    acc = vecmat(L[n], quad_bc<0>(pp[t]), sel16, acc); refill(n);
    acc = vecmat(L[n + 1], quad_bc<1>(pp[t]), sel16, acc); refill(n + 1);
    acc = vecmat(L[n + 2], quad_bc<2>(pp[t]), sel16, acc); refill(n + 2);
    acc = vecmat(L[n + 3], quad_bc<3>(pp[t]), sel16, acc); refill(n + 3);
  }
  u32 opr = (lane >> 3) * DH + acc_pos(16 * w + 2 * (lane & 7)), opw = w * DH + (r * 8 + c) * 4;
  u32 pub = lane < 8 ? WS_H1 + (h * NSPLIT + s) * REC1 + (8 * w + lane) * 16 : OOB;
  asm volatile("" : "+v"(pub), "+v"(opr), "+v"(opw));
  SB;
  {
    constexpr int n = 2 * NROW - NB;
    acc = vecmat(L[n], quad_bc<0>(pp[NBT - 1]), sel16, acc); acc = vecmat(L[n + 1], quad_bc<1>(pp[NBT - 1]), sel16, acc);
    acc = vecmat(L[n + 2], quad_bc<2>(pp[NBT - 1]), sel16, acc); acc = vecmat(L[n + 3], quad_bc<3>(pp[NBT - 1]), sel16, acc);
  }
  if (c < 8) *reinterpret_cast<f32x4*>(op_s + opw) = acc;
  if (w == 1) {
    auto valid = [&](u32 v) { return (v >> 12) == (tag & 0xfffffu) && (v & 0xfffu) >= TICKET0; };
    u32 tk = __builtin_amdgcn_readfirstlane(ticket);
    if (!valid(tk)) {
#pragma nounroll
      for (u32 spin = 0; spin < SPIN_CAP; ++spin) {
        __builtin_amdgcn_s_sleep(8);
        asm volatile("" ::: "memory");
        if (valid(__builtin_amdgcn_readfirstlane(__builtin_amdgcn_raw_buffer_load_b32(wsr, toff, 0, SC1)))) break;
      }
      tk = __builtin_amdgcn_readfirstlane(__builtin_amdgcn_raw_ptr_buffer_atomic_add_i32(1, wsr, toff, 0, 0));
    }
    if (lane == 0) *role_s = (tk & 0xfffu) - TICKET0;
  }
  __syncthreads();
  const u32 role = __builtin_amdgcn_readfirstlane(*role_s);
  st_unit(wsr, pub, oct_sum(op_s[opr]), oct_sum(op_s[opr + 4]), tag);
  if (role < NTILE) {
    const int hp = role / NSL, sp = role % NSL;
    const u32 rec = WS_H1 + (hp * NSPLIT + (c >> 1)) * REC1;
    const u32 oa = rec + (8 * w + 2 * r + (c & 1)) * 16, om = rec + 1024;
    const u32x4 nm = ld_unit(wsr, om);
    u32x4 WO[4], WD[2], na;
    u32x2 HG[2];
    u32 hoff[2];
    {
      const rsrc_t wor = mkrsrc(wo, NH * DH * DM * 2), wdr = mkrsrc(wd, FF * DM * 2);
      const u32 o = (u32)((hp * DH + 16 * w + 4 * r) * DM + sp * 128 + 8 * c) * 2;
#pragma unroll
      for (int i = 0; i < 2; ++i) {
        const int fr = 8 * w + 4 * i + r, f = hp * FROWS + (fr < FROWS ? fr : 0);
        hoff[i] = WS_HG + ((f >> 6) * 8 + (c & 7)) * 1024 + (c >> 3) * 512 + (f & 63) * 8;
        HG[i] = ld_gran(wsr, hoff[i]);
        WD[i] = bld<NT_>(wdr, fr < FROWS ? (u32)(f * DM + sp * 128 + 8 * c) * 2 : OOB, 0);
      }
#pragma unroll
      for (int i = 0; i < 4; ++i) WO[i] = bld<NT_>(wor, o + i * DM * 2, 0);
      SB;
      na = ld_unit(wsr, oa);
      SB;
    }
    if (!__all(HG[0][1] == tag && HG[1][1] == tag)) {
#pragma nounroll
      for (u32 spin = 0; spin < SPIN_CAP; ++spin) {
        asm volatile("" ::: "memory");
#pragma unroll
        for (int i = 0; i < 2; ++i) HG[i] = ld_gran(wsr, hoff[i]);
        if (__all(HG[0][1] == tag && HG[1][1] == tag)) break;
        __builtin_amdgcn_s_sleep(1);
      }
    }
    f32x4 y = zero4;
#pragma unroll
    for (int i = 0; i < 2; ++i) {
      float t = asf(HG[i][0]);
      t += dpp<XOR1>(t); t += dpp<XOR2>(t); t += dpp<HMIR>(t);
      const float o = dpp<ROR8>(t);
      const float g = rbf(c < 8 ? t : o), u = rbf(c < 8 ? o : t);
      const float hv = rbf(g * __builtin_amdgcn_rcpf(1.f + __expf(-g))) * u;
      y = vecmat(WD[i], pk_bf16(hv, hv), sel16, y);
    }
    const u32 yc = 16 * w + 2 * (lane & 7);
    u32 ypw = w * 128 + (r * 8 + c) * 4, ypr = (lane >> 3) * 128 + acc_pos(yc);
    u32 pub2 = lane < 8 ? WS_H2 + (sp * NH + hp) * 1024 + yc * 8 : OOB;
    asm volatile("" : "+v"(pub2), "+v"(ypw), "+v"(ypr));
    u32 m0, m1;
    {
      m0 = nm[0]; m1 = nm[2];
      const u64 need = __ballot(!fresh(nm, tag));
      if (need) {
        asm volatile("" : "+v"(na));
        poll_units<2>(wsr, om, tag, need, m0, m1);
        asm volatile("s_waitcnt vmcnt(0)" ::: "memory");
      }
    }
    float M = asf(m0);
    M = fmaxf(M, dpp<XOR2>(M)); M = fmaxf(M, dpp<ROR4>(M)); M = fmaxf(M, dpp<ROR8>(M));
    float f = __builtin_amdgcn_exp2f(asf(m0) - M), L = f * asf(m1);
    L += dpp<XOR2>(L); L += dpp<ROR4>(L); L += dpp<ROR8>(L);
    f *= __builtin_amdgcn_rcpf(L);
    asm volatile("" : "+v"(f));
    u32 a0, a1;
    {
      a0 = na[0]; a1 = na[2];
      const u64 need = __ballot(!fresh(na, tag));
      if (need) poll_units<2>(wsr, oa, tag, need, a0, a1);
    }
    float o0 = f * asf(a0), o1 = f * asf(a1);
    o0 += dpp<XOR2>(o0); o1 += dpp<XOR2>(o1);
    o0 += dpp<ROR4>(o0); o1 += dpp<ROR4>(o1);
    o0 += dpp<ROR8>(o0); o1 += dpp<ROR8>(o1);
    const u32 e0 = pk_bf16(o0, o0), e1 = pk_bf16(o1, o1), n0 = dppu<XOR1>(e0), n1 = dppu<XOR1>(e1);
    const bool odd = c & 1;
    const u32x4 b0 = diag(odd ? n0 : e0, sel16), b1 = diag(odd ? n1 : e1, sel16);
    const u32x4 b2 = diag(odd ? e0 : n0, sel16), b3 = diag(odd ? e1 : n1, sel16);
    f32x4 y2 = mm(WO[1], b1, zero4);
    y = mm(WO[0], b0, y); y2 = mm(WO[3], b3, y2); y = mm(WO[2], b2, y);
    if (c < 8) *reinterpret_cast<f32x4*>(yp_s + ypw) = y + y2;
    asm volatile("s_waitcnt lgkmcnt(0)\n\ts_barrier" ::: "memory");
    st_unit(wsr, pub2, oct_sum(yp_s[ypr]), oct_sum(yp_s[ypr + 4]), tag);
    asm volatile("s_waitcnt vmcnt(0)" ::: "memory");
  } else if (role < NTILE + NRED) {
    const int col = ((role - NTILE) * NW + w) * 4 + 2 * (lane & 1);
    const u32 off = WS_H2 + ((col >> 7) * NH + (lane >> 1)) * 1024 + (col & 127) * 8;
    const u32 xw = x32[col >> 1];
    u32 u0, u1;
    {
      const u32x4 n = ld_unit(wsr, off);
      u0 = n[0]; u1 = n[2];
      const u64 need = __ballot(!fresh(n, tag));
      asm volatile("" :: "v"(xw));
      if (need) poll_units<8>(wsr, off, tag, need, u0, u1);
    }
    float y0 = asf(u0), y1 = asf(u1);
    y0 += dpp<XOR2>(y0); y1 += dpp<XOR2>(y1);
    y0 += dpp<ROR4>(y0); y1 += dpp<ROR4>(y1);
    y0 = oct_sum(y0); y1 = oct_sum(y1);
    if (lane < 2) out32[col >> 1] = pk_bf16(lo16(xw) + y0, hi16(xw) + y1);
    if (role == NTILE && w == 0 && lane == 0)
      __hip_atomic_store((GAS u32*)(ws + WS_EPOCH), tag, __ATOMIC_RELAXED, __HIP_MEMORY_SCOPE_AGENT);
    asm volatile("s_waitcnt vmcnt(0)" ::: "memory");
  }
}

}

extern "C" void kernel_launch(void* const* d_in, const int* in_sizes, int n_in, void* d_out, int out_size,
                              void* d_ws, size_t ws_size, hipStream_t stream) {
  (void)in_sizes; (void)n_in; (void)out_size; (void)ws_size;
  fused_decode<<<NWG, NT, 0, stream>>>((const u32*)d_in[0], (const char*)d_in[1], (const char*)d_in[2],
                                       (const char*)d_in[3], (const char*)d_in[4], (const char*)d_in[6],
                                       (const char*)d_in[7], (const char*)d_in[8], (u32*)d_out, (char*)d_ws);
}
```

```cpp
#include <hip/hip_runtime.h>
#include <stdint.h>

namespace {

typedef unsigned u32;
typedef unsigned long long u64;
typedef u32 u32x4 __attribute__((ext_vector_type(4)));
typedef u32 u32x2 __attribute__((ext_vector_type(2)));
typedef float f32x2 __attribute__((ext_vector_type(2)));
typedef float f32x4 __attribute__((ext_vector_type(4)));
typedef __bf16 bf16x2 __attribute__((ext_vector_type(2)));
typedef __bf16 bf16x8 __attribute__((ext_vector_type(8)));
typedef __amdgpu_buffer_rsrc_t rsrc_t;
#define GAS __attribute__((address_space(1)))
#define CAS __attribute__((address_space(4)))
#define SB __builtin_amdgcn_sched_barrier(0)
#define PACE() do { __builtin_amdgcn_s_waitcnt((RING - 1) | 0x0F70); SB; } while (0)

constexpr int DM = 640, NH = 32, SK = 16384, DH = 128, FF = 1728;
constexpr int NSPLIT = 8, NWG = NH * NSPLIT, NW = 8, NT = NW * 64;
constexpr int KEYS_WG = SK / NSPLIT;
constexpr int NB = 4;
constexpr u32 KV_BYTES = KEYS_WG * DH * 2, BATCH = NW * NB * 1024;
constexpr int NBT = KV_BYTES / BATCH;
constexpr int NLD = NB * NBT;
constexpr int RING = 4;
constexpr int G1 = 4, G2 = 8, G3 = 11, NPRE = 16;
constexpr int QROWS = DM / NSPLIT;
constexpr int NSL = 5;
constexpr int FROWS = FF / NH;
constexpr int FLINES = FF / 64;
constexpr u32 NTILE = NH * NSL, NRED = DM / (4 * NW);
constexpr u32 OOB = 0x7ff00000u;
constexpr int NT_ = 2, SC1 = 16;

constexpr u32 WS_EPOCH = 0;
constexpr u32 WS_TICKET = 2048;
constexpr u32 WS_QP = 4096;
constexpr u32 WS_HG = WS_QP + NWG * 1024;
constexpr u32 REC1 = 1280;
constexpr u32 WS_H1 = WS_HG + FLINES * 8 * 1024;
constexpr u32 WS_H2 = WS_H1 + NWG * REC1;
constexpr u32 WS_END = WS_H2 + NSL * NH * 1024;
constexpr u32 TICKET0 = 0x400;
constexpr u32 SPIN_CAP = 1u << 18;

constexpr float LOG2E = 1.4426950408889634f;
constexpr float SCALE = 0.08838834764831845f;

__device__ __forceinline__ float asf(u32 x) { return __builtin_bit_cast(float, x); }
__device__ __forceinline__ u32 asu(float x) { return __builtin_bit_cast(u32, x); }
__device__ __forceinline__ float lo16(u32 x) { return asf(x << 16); }
__device__ __forceinline__ float hi16(u32 x) { return asf(x & 0xffff0000u); }
__device__ __forceinline__ u32 pk_bf16(float a, float b) { bf16x2 v = {(__bf16)a, (__bf16)b}; return __builtin_bit_cast(u32, v); }
__device__ __forceinline__ float rbf(float x) { return (float)(__bf16)x; }

template <int CTRL> __device__ __forceinline__ u32 dppu(u32 x) { return __builtin_amdgcn_mov_dpp(x, CTRL, 0xf, 0xf, true); }
template <int CTRL> __device__ __forceinline__ float dpp(float x) { return asf(dppu<CTRL>(asu(x))); }
constexpr int XOR1 = 0xB1, XOR2 = 0x4E, HMIR = 0x141, ROR4 = 0x124, ROR8 = 0x128;
constexpr int BC0 = 0x00, BC1 = 0x55, BC2 = 0xAA, BC3 = 0xFF;
__device__ __forceinline__ float row_sum(float x) {
  x += dpp<XOR1>(x); x += dpp<XOR2>(x); x += dpp<ROR4>(x); x += dpp<ROR8>(x); return x;
}
__device__ __forceinline__ float fold16(float a, float b) {
  auto s = __builtin_amdgcn_permlane16_swap(asu(a), asu(b), false, false);
  return asf(s[0]) + asf(s[1]);
}
__device__ __forceinline__ float fold32(float x) {
  auto s = __builtin_amdgcn_permlane32_swap(asu(x), asu(x), false, false);
  return asf(s[0]) + asf(s[1]);
}
__device__ __forceinline__ float xrow_sum(float x) { return fold32(fold16(x, x)); }
__device__ __forceinline__ float xrow_max(float x) {
  auto s = __builtin_amdgcn_permlane16_swap(asu(x), asu(x), false, false);
  x = fmaxf(asf(s[0]), asf(s[1]));
  auto t = __builtin_amdgcn_permlane32_swap(asu(x), asu(x), false, false);
  return fmaxf(asf(t[0]), asf(t[1]));
}
__device__ __forceinline__ float oct_sum(float x) { return xrow_sum(x + dpp<ROR8>(x)); }
__device__ __forceinline__ float oct_max(float x) { return xrow_max(fmaxf(x, dpp<ROR8>(x))); }

__device__ __forceinline__ f32x4 mm(u32x4 a, u32x4 b, f32x4 acc) {
  return __builtin_amdgcn_mfma_f32_16x16x32_bf16(__builtin_bit_cast(bf16x8, a), __builtin_bit_cast(bf16x8, b), acc, 0, 0, 0);
}
__device__ __forceinline__ u32x4 diag_sel(int j) {
  const u32 half = (j & 1) ? 0xffff0000u : 0x0000ffffu;
  return u32x4{(j >> 1) == 0 ? half : 0u, (j >> 1) == 1 ? half : 0u, (j >> 1) == 2 ? half : 0u, (j >> 1) == 3 ? half : 0u};
}
__device__ __forceinline__ u32x4 diag(u32 xx, u32x4 sel) { return u32x4{xx & sel[0], xx & sel[1], xx & sel[2], xx & sel[3]}; }
__device__ __forceinline__ f32x4 vecmat(u32x4 w, u32 xx, u32x4 sel, f32x4 acc) { return mm(w, diag(xx, sel), acc); }
__device__ __forceinline__ u32 acc_pos(u32 e) { return ((e >> 5) * 8 + (e & 7)) * 4 + ((e >> 3) & 3); }

__device__ __forceinline__ rsrc_t mkrsrc(const void* p, u32 bytes) {
  return __builtin_amdgcn_make_buffer_rsrc(const_cast<void*>(p), 0, bytes, 0x00020000);
}
template <int AUX> __device__ __forceinline__ u32x4 bld(rsrc_t r, u32 voff, u32 soff) {
  return __builtin_amdgcn_raw_buffer_load_b128(r, voff, soff, AUX);
}
template <int AUX = SC1> __device__ __forceinline__ void st_unit(rsrc_t r, u32 off, float a, float b, u32 tag) {
  __builtin_amdgcn_raw_buffer_store_b128(u32x4{asu(a), tag, asu(b), tag}, r, off, 0, AUX);
}
__device__ __forceinline__ u32x4 ld_unit(rsrc_t r, u32 off) { return bld<SC1>(r, off, 0); }
__device__ __forceinline__ u32x2 ld_gran(rsrc_t r, u32 off) { return __builtin_amdgcn_raw_buffer_load_b64(r, off, 0, SC1); }
__device__ __forceinline__ bool fresh(u32x4 u, u32 tag) { return u[1] == tag && u[3] == tag; }

#define POLL_GAP "s_sleep 5\n\t"
#define POLL_TAIL(L)                                        \
  "s_andn2_b64 %[need], %[need], vcc\n\t"                   \
  "s_cbranch_scc0 2f\n\t"                                   \
  "s_bcnt1_i32_b64 %[n], %[need]\n\t"                       \
  "s_cmp_le_u32 %[n], 8\n\t"                                \
  "s_cbranch_scc1 " L "f\n\t"                               \
  "s_sleep %[slp]\n"                                        \
  L ":\n\t"                                                 \
  "s_mov_b64 exec, %[need]\n\t"
#define POLL_LOOP_END                                       \
  "s_sub_u32 %[cnt], %[cnt], 1\n\t"                         \
  "s_cbranch_scc0 1b\n"                                     \
  "2:\n\t"                                                  \
  "s_nop 1"
#define POLL2_LD(A) "buffer_load_dwordx4 v[" #A ":" #A "+3], %[o], %[rs], 0 offen sc1\n\t"
#define POLL2_PASS(A, L)                                    \
  "s_waitcnt vmcnt(2)\n\t"                                  \
  "v_cmp_eq_u32 vcc, %[tag], v[" #A "+1]\n\t"               \
  "v_cmp_eq_u32 %[t], %[tag], v[" #A "+3]\n\t"              \
  "s_and_b64 vcc, vcc, %[t]\n\t"                            \
  "s_and_b64 vcc, vcc, %[need]\n\t"                         \
  "v_cndmask_b32 %[u0], %[u0], v[" #A "], vcc\n\t"          \
  "v_cndmask_b32 %[u1], %[u1], v[" #A "+2], vcc\n\t"        \
  POLL_TAIL(L) POLL2_LD(A) "s_mov_b64 exec, -1\n\t"
template <int SLP> __device__ __forceinline__ void poll_units(rsrc_t rs, u32 o, u32 tag, u64 need, u32& u0, u32& u1) {
  u64 t; u32 n, cnt = SPIN_CAP;
  asm volatile(
      "s_mov_b64 exec, %[need]\n\t" POLL2_LD(208) POLL_GAP POLL2_LD(212) POLL_GAP POLL2_LD(216) "s_mov_b64 exec, -1\n"
      "1:\n\t" POLL2_PASS(208, "3") POLL2_PASS(212, "4") POLL2_PASS(216, "5") POLL_LOOP_END
      : [u0] "+v"(u0), [u1] "+v"(u1), [need] "+s"(need), [cnt] "+s"(cnt), [t] "=&s"(t), [n] "=&s"(n)
      : [o] "v"(o), [rs] "s"(rs), [tag] "s"(tag), [slp] "i"(SLP)
      : "vcc", "scc", "memory", "v208", "v209", "v210", "v211", "v212", "v213", "v214", "v215", "v216", "v217", "v218", "v219");
}

__device__ __forceinline__ float quad_trace(f32x4 d) {
  float t = ((dpp<BC0>(d[0]) + dpp<BC1>(d[1])) + dpp<BC2>(d[2])) + dpp<BC3>(d[3]);
  asm("" : "+v"(t));
  return t;
}
__device__ __forceinline__ float batch_score(const f32x4 (&d)[NB], int slot) {
  const float t0 = quad_trace(d[0]), t1 = quad_trace(d[1]), t2 = quad_trace(d[2]), t3 = quad_trace(d[3]);
  const float t = (slot & 2) ? ((slot & 1) ? t3 : t2) : ((slot & 1) ? t1 : t0);
  return rbf(rbf(t) * SCALE) * LOG2E;
}
template <int I> __device__ __forceinline__ u32 quad_bc(u32 x) { return dppu<I == 0 ? BC0 : I == 1 ? BC1 : I == 2 ? BC2 : BC3>(x); }

__global__ void __launch_bounds__(NT, 2)
fused_decode(const u32* __restrict__ x32, const char* __restrict__ kc, const char* __restrict__ vc,
             const char* __restrict__ wq, const char* __restrict__ wo, const char* __restrict__ wg,
             const char* __restrict__ wu, const char* __restrict__ wd, u32* __restrict__ out32, char* ws) {
  __shared__ __attribute__((aligned(16))) float lds[5 * NW * 128 + 64 + 2 * NW + 4];
  u32* xn_s = reinterpret_cast<u32*>(lds);
  float* qp_s = lds + NW * 128;
  float* fp_s = qp_s + NW * 128;
  float* op_s = fp_s + NW * 128;
  float* yp_s = op_s + NW * 128;
  u32* q_s = reinterpret_cast<u32*>(yp_s + NW * 128);
  float* ml_s = yp_s + NW * 128 + 64;
  u32* role_s = reinterpret_cast<u32*>(ml_s + 2 * NW);

  asm volatile("" :: "s"(x32), "s"(kc), "s"(vc), "s"(wq), "s"(wo), "s"(wg), "s"(wu), "s"(wd), "s"(out32), "s"(ws));
  const int tid = threadIdx.x, lane = tid & 63, r = lane >> 4, c = lane & 15;
  const int w = __builtin_amdgcn_readfirstlane(tid >> 6);
  const int b = blockIdx.x, xs = b & 7, j = b >> 3;
  const int h = xs * 4 + (j >> 3), s = j & 7;
  const rsrc_t wsr = mkrsrc(ws, WS_END);
  const f32x4 zero4 = {0.f, 0.f, 0.f, 0.f};

  const int qrow = s * QROWS + 4 * w;
  const bool ffn_on = b < FLINES * 8;
  const int frow = xs * 80 + 8 * (w & 3);
  const size_t kv0 = (size_t)(h * NSPLIT + s) * KV_BYTES;
  const rsrc_t kr = mkrsrc(kc + kv0, KV_BYTES), vr = mkrsrc(vc + kv0, KV_BYTES);
  const u32 vo = lane * 16 + w * NB * 1024;
  const u32 ko = (c >> 2) * 256 + (c & 3) * 64 + r * 16 + w * NB * 1024;
  u32 bo = ((u32)(h * NSPLIT + s) / NB) * BATCH - BATCH;
  u32x4 L[2 * NLD];
  auto request = [&](int n) {
    if (n % NB == 0) { bo = (bo + BATCH) & (KV_BYTES - 1); asm volatile("" : "+s"(bo)); }
    L[n] = n < NLD ? bld<NT_>(kr, ko + (n % NB) * 1024, bo) : bld<NT_>(vr, vo + (n % NB) * 1024, bo);
  };
  auto request_range = [&](int n0, int n1) {
#pragma unroll
    for (int n = n0; n < n1; ++n) request(n);
    SB;
  };
  auto request_paced = [&](int n0, int n1) {
#pragma unroll
    for (int n = n0; n < n1; ++n) { PACE(); request(n); SB; }
  };
  const rsrc_t xrs = mkrsrc(x32, DM * 2);
  const u32x4 xa = bld<0>(xrs, lane * 16, 0), xb = bld<0>(xrs, lane * 16 + 1024, 0);
  u32 tag = *(const CAS u32*)(ws + WS_EPOCH) + 1u;
  u32x4 WQ[3];
  {
    const rsrc_t qr = mkrsrc(wq, DM * NH * DH * 2);
    const u32 o = (u32)((qrow + r) * (NH * DH) + h * DH + 8 * c) * 2;
    WQ[0] = bld<NT_>(qr, o, 0);
    WQ[1] = bld<NT_>(qr, o + 32 * NH * DH * 2, 0);
    WQ[2] = bld<NT_>(qr, w < 4 ? o + 64 * NH * DH * 2 : OOB, 0);
    SB;
  }
  request_range(0, G1);

  {
    float xv[16], sum = 0.f;
#pragma unroll
    for (int i = 0; i < 4; ++i) { xv[2 * i] = lo16(xa[i]); xv[2 * i + 1] = hi16(xa[i]); xv[8 + 2 * i] = lo16(xb[i]); xv[9 + 2 * i] = hi16(xb[i]); }
#pragma unroll
    for (int i = 0; i < 16; ++i) sum += xv[i];
    const float mean = xrow_sum(row_sum(sum)) * (1.f / DM);
    float sq = 0.f, sq2 = 0.f;
#pragma unroll
    for (int i = 0; i < 8; ++i) { const float d = xv[i] - mean, e = xv[8 + i] - mean; sq = fmaf(d, d, sq); sq2 = fmaf(e, e, sq2); }
    const float var = xrow_sum(row_sum(lane < 16 ? sq + sq2 : sq)) * (1.f / DM);
    const float mb = rbf(mean);
    const float inv = __builtin_amdgcn_rcpf(rbf(__builtin_amdgcn_sqrtf(rbf(rbf(var) + 1e-5f))));
    u32 n[16];
#pragma unroll
    for (int i = 0; i < 16; ++i) { const float t = rbf(xv[i] - mb) * inv; n[i] = pk_bf16(t, t); }
    *reinterpret_cast<u32x4*>(xn_s + 8 * lane) = u32x4{n[0], n[1], n[2], n[3]};
    *reinterpret_cast<u32x4*>(xn_s + 8 * lane + 4) = u32x4{n[4], n[5], n[6], n[7]};
    *reinterpret_cast<u32x4*>(xn_s + 512 + 8 * lane) = u32x4{n[8], n[9], n[10], n[11]};
    *reinterpret_cast<u32x4*>(xn_s + 512 + 8 * lane + 4) = u32x4{n[12], n[13], n[14], n[15]};
  }
  SB;
  request_range(G1, G2);

  const u32x4 sel16 = diag_sel(c);
  {
    f32x4 a = zero4;
#pragma unroll
    for (int i = 0; i < 3; ++i) a = vecmat(WQ[i], xn_s[qrow + 32 * i + r], sel16, a);
    if (c < 8) *reinterpret_cast<f32x4*>(qp_s + w * 128 + (r * 8 + c) * 4) = a;
  }
  asm volatile("" : "+s"(tag));
  __syncthreads();
  if (w == 0) {
    const float* p = qp_s + acc_pos(2 * lane);
    float t0 = 0.f, t1 = 0.f;
#pragma unroll
    for (int i = 0; i < NW; ++i) { t0 += p[i * 128]; t1 += p[i * 128 + 4]; }
    st_unit(wsr, WS_QP + (h * NSPLIT + s) * 1024 + lane * 16, t0, t1, tag);
    if (b == 0 && lane == 0)
      __hip_atomic_store((GAS u32*)(ws + WS_TICKET), (tag << 12) + TICKET0, __ATOMIC_RELAXED, __HIP_MEMORY_SCOPE_AGENT);
  }
  SB;

  u32x4 WF[3];
  {
    const rsrc_t fr = mkrsrc(w < 4 ? wg : wu, ffn_on ? DM * FF * 2 : 0);
    const u32 o = (u32)((frow + (lane >> 3)) * FF + j * 64 + 8 * (lane & 7)) * 2;
    PACE(); WF[0] = bld<NT_>(fr, o, 0); SB;
    PACE(); WF[1] = bld<NT_>(fr, o + 32 * FF * 2, 0); SB;
    PACE(); WF[2] = bld<NT_>(fr, (w & 3) < 2 ? o + 64 * FF * 2 : OOB, 0);
    SB;
  }
  request_paced(G2, G3);
  const u32 qoff = WS_QP + h * NSPLIT * 1024 + lane * 16;
  u32x4 qu[NSPLIT];
  if (w == 0) {
#pragma unroll
    for (int i = 0; i < NSPLIT; ++i) qu[i] = ld_unit(wsr, qoff + i * 1024);
  }
  SB;
  request_paced(G3, NPRE);
  if (w == 0) {
    auto all_fresh = [&]() {
      bool ok = true;
#pragma unroll
      for (int i = 0; i < NSPLIT; ++i) ok = ok && fresh(qu[i], tag);
      return __all(ok);
    };
    if (!all_fresh()) {
#pragma nounroll
      for (u32 spin = 0; spin < SPIN_CAP; ++spin) {
        asm volatile("" ::: "memory");
#pragma unroll
        for (int i = 0; i < NSPLIT; ++i) qu[i] = ld_unit(wsr, qoff + i * 1024);
        if (all_fresh()) break;
      }
    }
    float t0 = 0.f, t1 = 0.f;
#pragma unroll
    for (int i = 0; i < NSPLIT; ++i) { t0 += asf(qu[i][0]); t1 += asf(qu[i][2]); }
    q_s[lane] = pk_bf16(t0, t1);
  }
  {
    const u32x4 sel8 = diag_sel(lane & 7);
    f32x4 f = zero4;
#pragma unroll
    for (int i = 0; i < 3; ++i) f = vecmat(WF[i], xn_s[frow + 32 * i + (lane >> 3)], sel8, f);
    if ((c >> 3) == (r >> 1)) *reinterpret_cast<f32x4*>(fp_s + w * 128 + (r * 8 + (c & 7)) * 4) = f;
  }
  __syncthreads();
  const u32x4 q = *reinterpret_cast<const u32x4*>(q_s + 16 * (c & 3) + 4 * r);
  if (w == NW - 1 && ffn_on) {
    const float* p = fp_s + (lane >> 5) * 512 + acc_pos(2 * (lane & 31));
    float g0 = 0.f, g1 = 0.f;
#pragma unroll
    for (int i = 0; i < 8; ++i) { g0 += p[i * 64]; g1 += p[i * 64 + 4]; }
    st_unit<SC1 | NT_>(wsr, WS_HG + b * 1024 + lane * 16, g0, g1, tag);
  }

  auto refill = [&](int n) {
    const int nx = n < RING ? NPRE + n : n >= NPRE ? n + RING : 2 * NLD;
    if (nx < 2 * NLD) { SB; request(nx); SB; }
  };
  float sv[NBT];
#pragma unroll
  for (int t = 0; t < NBT; ++t) {
    f32x4 d[NB];
#pragma unroll
    for (int i = 0; i < NB; ++i) { d[i] = mm(L[NB * t + i], q, zero4); refill(NB * t + i); }
    sv[t] = batch_score(d, c & 3);
    asm volatile("" : "+v"(sv[t]));
  }
  float mx = sv[0];
#pragma unroll
  for (int t = 1; t < NBT; ++t) mx = fmaxf(mx, sv[t]);
  mx = fmaxf(mx, dpp<XOR1>(mx));
  mx = xrow_max(fmaxf(mx, dpp<XOR2>(mx)));
  float pw[NBT], ls = 0.f;
#pragma unroll
  for (int t = 0; t < NBT; ++t) { pw[t] = __builtin_amdgcn_exp2f(sv[t] - mx); ls += pw[t]; }
  ls += dpp<XOR1>(ls);
  ls = xrow_sum(ls + dpp<XOR2>(ls));
  if (lane == 0) *reinterpret_cast<f32x2*>(ml_s + 2 * w) = f32x2{mx, ls};
  asm volatile("s_waitcnt lgkmcnt(0)\n\ts_barrier" ::: "memory");
  u32 pp[NBT];
  {
    const f32x2 ml = *reinterpret_cast<const f32x2*>(ml_s + 2 * (lane >> 3));
    const float M = oct_max(ml[0]), L = oct_sum(__builtin_amdgcn_exp2f(ml[0] - M) * ml[1]);
    st_unit<SC1 | NT_>(wsr, (w == 0 && lane == 0) ? WS_H1 + (h * NSPLIT + s) * REC1 + 1024 : OOB, M, L, tag);
    const float f = __builtin_amdgcn_exp2f(mx - M);
#pragma unroll
    for (int t = 0; t < NBT; ++t) { const float p = pw[t] * f; pp[t] = pk_bf16(p, p); }
  }
  const u32 toff = (w == 1 && lane == 0) ? WS_TICKET : OOB;
  u32 ticket = 0;
  f32x4 acc = zero4;
#pragma unroll
  for (int t = 0; t < NBT - 1; ++t) {
    if (t == NBT - 2) { SB; ticket = __builtin_amdgcn_raw_ptr_buffer_atomic_add_i32(1, wsr, toff, 0, 0); SB; }
    const int n = NLD + NB * t;
    acc = vecmat(L[n], quad_bc<0>(pp[t]), sel16, acc); refill(n);
    acc = vecmat(L[n + 1], quad_bc<1>(pp[t]), sel16, acc); refill(n + 1);
    acc = vecmat(L[n + 2], quad_bc<2>(pp[t]), sel16, acc); refill(n + 2);
    acc = vecmat(L[n + 3], quad_bc<3>(pp[t]), sel16, acc); refill(n + 3);
  }
  u32 opr = (lane >> 3) * DH + acc_pos(16 * w + 2 * (lane & 7)), opw = w * DH + (r * 8 + c) * 4;
  u32 pub = lane < 8 ? WS_H1 + (h * NSPLIT + s) * REC1 + (8 * w + lane) * 16 : OOB;
  asm volatile("" : "+v"(pub), "+v"(opr), "+v"(opw));
  SB;
  {
    constexpr int n = 2 * NLD - NB;
    acc = vecmat(L[n], quad_bc<0>(pp[NBT - 1]), sel16, acc); acc = vecmat(L[n + 1], quad_bc<1>(pp[NBT - 1]), sel16, acc);
    acc = vecmat(L[n + 2], quad_bc<2>(pp[NBT - 1]), sel16, acc); acc = vecmat(L[n + 3], quad_bc<3>(pp[NBT - 1]), sel16, acc);
  }
  if (c < 8) *reinterpret_cast<f32x4*>(op_s + opw) = acc;
  if (w == 1) {
    auto valid = [&](u32 v) { return (v >> 12) == (tag & 0xfffffu) && (v & 0xfffu) >= TICKET0; };
    u32 tk = __builtin_amdgcn_readfirstlane(ticket);
    if (!valid(tk)) {
#pragma nounroll
      for (u32 spin = 0; spin < SPIN_CAP; ++spin) {
        __builtin_amdgcn_s_sleep(8);
        asm volatile("" ::: "memory");
        if (valid(__builtin_amdgcn_readfirstlane(__builtin_amdgcn_raw_buffer_load_b32(wsr, toff, 0, SC1)))) break;
      }
      tk = __builtin_amdgcn_readfirstlane(__builtin_amdgcn_raw_ptr_buffer_atomic_add_i32(1, wsr, toff, 0, 0));
    }
    if (lane == 0) *role_s = (tk & 0xfffu) - TICKET0;
  }
  __syncthreads();
  const u32 role = __builtin_amdgcn_readfirstlane(*role_s);
  st_unit(wsr, pub, oct_sum(op_s[opr]), oct_sum(op_s[opr + 4]), tag);
  if (role < NTILE) {
    const int hp = role / NSL, sp = role % NSL;
    const u32 rec = WS_H1 + (hp * NSPLIT + (c >> 1)) * REC1;
    const u32 oa = rec + (8 * w + 2 * r + (c & 1)) * 16, om = rec + 1024;
    const u32x4 nm = ld_unit(wsr, om);
    u32x4 WO[4], WD[2], na;
    u32x2 HG[2];
    u32 hoff[2];
    {
      const rsrc_t wor = mkrsrc(wo, NH * DH * DM * 2), wdr = mkrsrc(wd, FF * DM * 2);
      const u32 o = (u32)((hp * DH + 16 * w + 4 * r) * DM + sp * 128 + 8 * c) * 2;
#pragma unroll
      for (int i = 0; i < 2; ++i) {
        const int fr = 8 * w + 4 * i + r, f = hp * FROWS + (fr < FROWS ? fr : 0);
        hoff[i] = WS_HG + ((f >> 6) * 8 + (c & 7)) * 1024 + (c >> 3) * 512 + (f & 63) * 8;
        HG[i] = ld_gran(wsr, hoff[i]);
        WD[i] = bld<NT_>(wdr, fr < FROWS ? (u32)(f * DM + sp * 128 + 8 * c) * 2 : OOB, 0);
      }
#pragma unroll
      for (int i = 0; i < 4; ++i) WO[i] = bld<NT_>(wor, o + i * DM * 2, 0);
      SB;
      na = ld_unit(wsr, oa);
      SB;
    }
    if (!__all(HG[0][1] == tag && HG[1][1] == tag)) {
#pragma nounroll
      for (u32 spin = 0; spin < SPIN_CAP; ++spin) {
        asm volatile("" ::: "memory");
#pragma unroll
        for (int i = 0; i < 2; ++i) HG[i] = ld_gran(wsr, hoff[i]);
        if (__all(HG[0][1] == tag && HG[1][1] == tag)) break;
        __builtin_amdgcn_s_sleep(1);
      }
    }
    f32x4 y = zero4;
#pragma unroll
    for (int i = 0; i < 2; ++i) {
      float t = asf(HG[i][0]);
      t += dpp<XOR1>(t); t += dpp<XOR2>(t); t += dpp<HMIR>(t);
      const float o = dpp<ROR8>(t);
      const float g = rbf(c < 8 ? t : o), u = rbf(c < 8 ? o : t);
      const float hv = rbf(g * __builtin_amdgcn_rcpf(1.f + __expf(-g))) * u;
      y = vecmat(WD[i], pk_bf16(hv, hv), sel16, y);
    }
    const u32 yc = 16 * w + 2 * (lane & 7);
    u32 ypw = w * 128 + (r * 8 + c) * 4, ypr = (lane >> 3) * 128 + acc_pos(yc);
    u32 pub2 = lane < 8 ? WS_H2 + (sp * NH + hp) * 1024 + yc * 8 : OOB;
    asm volatile("" : "+v"(pub2), "+v"(ypw), "+v"(ypr));
    u32 m0, m1;
    {
      m0 = nm[0]; m1 = nm[2];
      const u64 need = __ballot(!fresh(nm, tag));
      if (need) {
        asm volatile("" : "+v"(na));
        poll_units<2>(wsr, om, tag, need, m0, m1);
        asm volatile("s_waitcnt vmcnt(0)" ::: "memory");
      }
    }
    float M = asf(m0);
    M = fmaxf(M, dpp<XOR2>(M)); M = fmaxf(M, dpp<ROR4>(M)); M = fmaxf(M, dpp<ROR8>(M));
    float f = __builtin_amdgcn_exp2f(asf(m0) - M), L = f * asf(m1);
    L += dpp<XOR2>(L); L += dpp<ROR4>(L); L += dpp<ROR8>(L);
    f *= __builtin_amdgcn_rcpf(L);
    asm volatile("" : "+v"(f));
    u32 a0, a1;
    {
      a0 = na[0]; a1 = na[2];
      const u64 need = __ballot(!fresh(na, tag));
      if (need) poll_units<2>(wsr, oa, tag, need, a0, a1);
    }
    float o0 = f * asf(a0), o1 = f * asf(a1);
    o0 += dpp<XOR2>(o0); o1 += dpp<XOR2>(o1);
    o0 += dpp<ROR4>(o0); o1 += dpp<ROR4>(o1);
    o0 += dpp<ROR8>(o0); o1 += dpp<ROR8>(o1);
    const u32 e0 = pk_bf16(o0, o0), e1 = pk_bf16(o1, o1), n0 = dppu<XOR1>(e0), n1 = dppu<XOR1>(e1);
    const bool odd = c & 1;
    const u32x4 b0 = diag(odd ? n0 : e0, sel16), b1 = diag(odd ? n1 : e1, sel16);
    const u32x4 b2 = diag(odd ? e0 : n0, sel16), b3 = diag(odd ? e1 : n1, sel16);
    f32x4 y2 = mm(WO[1], b1, zero4);
    y = mm(WO[0], b0, y); y2 = mm(WO[3], b3, y2); y = mm(WO[2], b2, y);
    if (c < 8) *reinterpret_cast<f32x4*>(yp_s + ypw) = y + y2;
    asm volatile("s_waitcnt lgkmcnt(0)\n\ts_barrier" ::: "memory");
    st_unit(wsr, pub2, oct_sum(yp_s[ypr]), oct_sum(yp_s[ypr + 4]), tag);
    asm volatile("s_waitcnt vmcnt(0)" ::: "memory");
  } else if (role < NTILE + NRED) {
    const int col = ((role - NTILE) * NW + w) * 4 + 2 * (lane & 1);
    const u32 off = WS_H2 + ((col >> 7) * NH + (lane >> 1)) * 1024 + (col & 127) * 8;
    const u32 xw = x32[col >> 1];
    u32 u0, u1;
    {
      const u32x4 n = ld_unit(wsr, off);
      u0 = n[0]; u1 = n[2];
      const u64 need = __ballot(!fresh(n, tag));
      asm volatile("" :: "v"(xw));
      if (need) poll_units<8>(wsr, off, tag, need, u0, u1);
    }
    float y0 = asf(u0), y1 = asf(u1);
    y0 += dpp<XOR2>(y0); y1 += dpp<XOR2>(y1);
    y0 += dpp<ROR4>(y0); y1 += dpp<ROR4>(y1);
    y0 = oct_sum(y0); y1 = oct_sum(y1);
    if (lane < 2) out32[col >> 1] = pk_bf16(lo16(xw) + y0, hi16(xw) + y1);
    if (role == NTILE && w == 0 && lane == 0)
      __hip_atomic_store((GAS u32*)(ws + WS_EPOCH), tag, __ATOMIC_RELAXED, __HIP_MEMORY_SCOPE_AGENT);
    asm volatile("s_waitcnt vmcnt(0)" ::: "memory");
  }
}

}

extern "C" void kernel_launch(void* const* d_in, const int* in_sizes, int n_in, void* d_out, int out_size,
                              void* d_ws, size_t ws_size, hipStream_t stream) {
  (void)in_sizes; (void)n_in; (void)out_size; (void)ws_size;
  fused_decode<<<NWG, NT, 0, stream>>>((const u32*)d_in[0], (const char*)d_in[1], (const char*)d_in[2],
                                       (const char*)d_in[3], (const char*)d_in[4], (const char*)d_in[6],
                                       (const char*)d_in[7], (const char*)d_in[8], (u32*)d_out, (char*)d_ws);
}
```

```cpp
#include <hip/hip_runtime.h>
#include <stdint.h>

namespace {

typedef unsigned u32;
typedef unsigned long long u64;
typedef u32 u32x4 __attribute__((ext_vector_type(4)));
typedef u32 u32x2 __attribute__((ext_vector_type(2)));
typedef float f32x2 __attribute__((ext_vector_type(2)));
typedef float f32x4 __attribute__((ext_vector_type(4)));
typedef __bf16 bf16x2 __attribute__((ext_vector_type(2)));
typedef __bf16 bf16x8 __attribute__((ext_vector_type(8)));
typedef __amdgpu_buffer_rsrc_t rsrc_t;
#define GAS __attribute__((address_space(1)))
#define CAS __attribute__((address_space(4)))
#define SB __builtin_amdgcn_sched_barrier(0)
#define PACE() do { __builtin_amdgcn_s_waitcnt((RING - 1) | 0x0F70); SB; } while (0)

constexpr int DM = 640, NH = 32, SK = 16384, DH = 128, FF = 1728;
constexpr int NSPLIT = 8, NWG = NH * NSPLIT, NW = 8, NT = NW * 64;
constexpr int KEYS_WG = SK / NSPLIT;
constexpr int NB = 4;
constexpr u32 KV_BYTES = KEYS_WG * DH * 2, BATCH = NW * NB * 1024;
constexpr int NBT = KV_BYTES / BATCH;
constexpr int NLD = NB * NBT;
constexpr int RING = 4;
constexpr int G1 = 4, G2 = 8, G3 = 11, NPRE = 16;
constexpr int QROWS = DM / NSPLIT;
constexpr int NSL = 5;
constexpr int FROWS = FF / NH;
constexpr int FLINES = FF / 64;
constexpr u32 NTILE = NH * NSL, NRED = DM / (4 * NW);
constexpr u32 OOB = 0x7ff00000u;
constexpr int NT_ = 2, SC1 = 16;

constexpr u32 WS_EPOCH = 0;
constexpr u32 WS_TICKET = 2048;
constexpr u32 WS_QP = 4096;
constexpr u32 WS_HG = WS_QP + NWG * 1024;
constexpr u32 REC1 = 1280;
constexpr u32 WS_H1 = WS_HG + FLINES * 8 * 1024;
constexpr u32 WS_H2 = WS_H1 + NWG * REC1;
constexpr u32 WS_END = WS_H2 + NSL * NH * 1024;
constexpr u32 TICKET0 = 0x400;
constexpr u32 SPIN_CAP = 1u << 18;

constexpr float LOG2E = 1.4426950408889634f;
constexpr float SCALE = 0.08838834764831845f;

__device__ __forceinline__ float asf(u32 x) { return __builtin_bit_cast(float, x); }
__device__ __forceinline__ u32 asu(float x) { return __builtin_bit_cast(u32, x); }
__device__ __forceinline__ float lo16(u32 x) { return asf(x << 16); }
__device__ __forceinline__ float hi16(u32 x) { return asf(x & 0xffff0000u); }
__device__ __forceinline__ u32 pk_bf16(float a, float b) { bf16x2 v = {(__bf16)a, (__bf16)b}; return __builtin_bit_cast(u32, v); }
__device__ __forceinline__ float rbf(float x) { return (float)(__bf16)x; }

template <int CTRL> __device__ __forceinline__ u32 dppu(u32 x) { return __builtin_amdgcn_mov_dpp(x, CTRL, 0xf, 0xf, true); }
template <int CTRL> __device__ __forceinline__ float dpp(float x) { return asf(dppu<CTRL>(asu(x))); }
constexpr int XOR1 = 0xB1, XOR2 = 0x4E, HMIR = 0x141, ROR4 = 0x124, ROR8 = 0x128;
constexpr int BC0 = 0x00, BC1 = 0x55, BC2 = 0xAA, BC3 = 0xFF;
__device__ __forceinline__ float row_sum(float x) {
  x += dpp<XOR1>(x); x += dpp<XOR2>(x); x += dpp<ROR4>(x); x += dpp<ROR8>(x); return x;
}
__device__ __forceinline__ float fold16(float a, float b) {
  auto s = __builtin_amdgcn_permlane16_swap(asu(a), asu(b), false, false);
  return asf(s[0]) + asf(s[1]);
}
__device__ __forceinline__ float fold32(float x) {
  auto s = __builtin_amdgcn_permlane32_swap(asu(x), asu(x), false, false);
  return asf(s[0]) + asf(s[1]);
}
__device__ __forceinline__ float xrow_sum(float x) { return fold32(fold16(x, x)); }
__device__ __forceinline__ float xrow_max(float x) {
  auto s = __builtin_amdgcn_permlane16_swap(asu(x), asu(x), false, false);
  x = fmaxf(asf(s[0]), asf(s[1]));
  auto t = __builtin_amdgcn_permlane32_swap(asu(x), asu(x), false, false);
  return fmaxf(asf(t[0]), asf(t[1]));
}
__device__ __forceinline__ float oct_sum(float x) { return xrow_sum(x + dpp<ROR8>(x)); }
__device__ __forceinline__ float oct_max(float x) { return xrow_max(fmaxf(x, dpp<ROR8>(x))); }

__device__ __forceinline__ f32x4 mm(u32x4 a, u32x4 b, f32x4 acc) {
  return __builtin_amdgcn_mfma_f32_16x16x32_bf16(__builtin_bit_cast(bf16x8, a), __builtin_bit_cast(bf16x8, b), acc, 0, 0, 0);
}
__device__ __forceinline__ u32x4 diag_sel(int j) {
  const u32 half = (j & 1) ? 0xffff0000u : 0x0000ffffu;
  return u32x4{(j >> 1) == 0 ? half : 0u, (j >> 1) == 1 ? half : 0u, (j >> 1) == 2 ? half : 0u, (j >> 1) == 3 ? half : 0u};
}
__device__ __forceinline__ u32x4 diag(u32 xx, u32x4 sel) { return u32x4{xx & sel[0], xx & sel[1], xx & sel[2], xx & sel[3]}; }
__device__ __forceinline__ f32x4 vecmat(u32x4 w, u32 xx, u32x4 sel, f32x4 acc) { return mm(w, diag(xx, sel), acc); }
__device__ __forceinline__ u32 acc_pos(u32 e) { return ((e >> 5) * 8 + (e & 7)) * 4 + ((e >> 3) & 3); }

__device__ __forceinline__ rsrc_t mkrsrc(const void* p, u32 bytes) {
  return __builtin_amdgcn_make_buffer_rsrc(const_cast<void*>(p), 0, bytes, 0x00020000);
}
template <int AUX> __device__ __forceinline__ u32x4 bld(rsrc_t r, u32 voff, u32 soff) {
  return __builtin_amdgcn_raw_buffer_load_b128(r, voff, soff, AUX);
}
template <int AUX = SC1> __device__ __forceinline__ void st_unit(rsrc_t r, u32 off, float a, float b, u32 tag) {
  __builtin_amdgcn_raw_buffer_store_b128(u32x4{asu(a), tag, asu(b), tag}, r, off, 0, AUX);
}
__device__ __forceinline__ u32x4 ld_unit(rsrc_t r, u32 off) { return bld<SC1>(r, off, 0); }
__device__ __forceinline__ u32x2 ld_gran(rsrc_t r, u32 off) { return __builtin_amdgcn_raw_buffer_load_b64(r, off, 0, SC1); }
__device__ __forceinline__ bool fresh(u32x4 u, u32 tag) { return u[1] == tag && u[3] == tag; }

#define POLL_LD(A) "buffer_load_dwordx4 v[" #A ":" #A "+3], %[o], %[rs], 0 offen sc1\n\t"
#define POLL_PASS(A, YOUNGER, L)                            \
  "s_waitcnt vmcnt(" #YOUNGER ")\n\t"                       \
  "v_cmp_eq_u32 vcc, %[tag], v[" #A "+1]\n\t"               \
  "v_cmp_eq_u32 %[t], %[tag], v[" #A "+3]\n\t"              \
  "s_and_b64 vcc, vcc, %[t]\n\t"                            \
  "s_and_b64 vcc, vcc, %[need]\n\t"                         \
  "v_cndmask_b32 %[u0], %[u0], v[" #A "], vcc\n\t"          \
  "v_cndmask_b32 %[u1], %[u1], v[" #A "+2], vcc\n\t"        \
  "s_andn2_b64 %[need], %[need], vcc\n\t"                   \
  "s_cbranch_scc0 2f\n\t"                                   \
  "s_bcnt1_i32_b64 %[n], %[need]\n\t"                       \
  "s_cmp_le_u32 %[n], 8\n\t"                                \
  "s_cbranch_scc1 " L "f\n\t"                               \
  "s_sleep %[slp]\n"                                        \
  L ":\n\t"                                                 \
  "s_mov_b64 exec, %[need]\n\t"                             \
  POLL_LD(A)                                                \
  "s_mov_b64 exec, -1\n\t"
#define POLL_END                                            \
  "s_sub_u32 %[cnt], %[cnt], 1\n\t"                         \
  "s_cbranch_scc0 1b\n"                                     \
  "2:\n\t"                                                  \
  "s_nop 1"
#define POLL_OPERANDS                                                                                     \
      : [u0] "+v"(u0), [u1] "+v"(u1), [need] "+s"(need), [cnt] "+s"(cnt), [t] "=&s"(t), [n] "=&s"(n)      \
      : [o] "v"(o), [rs] "s"(rs), [tag] "s"(tag), [slp] "i"(SLP)                                          \
      : "vcc", "scc", "memory", "v208", "v209", "v210", "v211", "v212", "v213", "v214", "v215", "v216",   \
        "v217", "v218", "v219"
template <int DEPTH, int SLP>
__device__ __forceinline__ void poll_units(rsrc_t rs, u32 o, u32 tag, u64 need, u32& u0, u32& u1) {
  u64 t; u32 n, cnt = SPIN_CAP;
  if constexpr (DEPTH == 3)
    asm volatile("s_mov_b64 exec, %[need]\n\t" POLL_LD(208) "s_sleep 5\n\t" POLL_LD(212) "s_sleep 5\n\t" POLL_LD(216)
                 "s_mov_b64 exec, -1\n1:\n\t" POLL_PASS(208, 2, "3") POLL_PASS(212, 2, "4") POLL_PASS(216, 2, "5") POLL_END
                 POLL_OPERANDS);
  else
    asm volatile("s_mov_b64 exec, %[need]\n\t" POLL_LD(208) "s_sleep 6\n\t" POLL_LD(212)
                 "s_mov_b64 exec, -1\n1:\n\t" POLL_PASS(208, 1, "3") POLL_PASS(212, 1, "4") POLL_END
                 POLL_OPERANDS);
}

__device__ __forceinline__ float quad_trace(f32x4 d) {
  float t = ((dpp<BC0>(d[0]) + dpp<BC1>(d[1])) + dpp<BC2>(d[2])) + dpp<BC3>(d[3]);
  asm("" : "+v"(t));
  return t;
}
__device__ __forceinline__ float batch_score(const f32x4 (&d)[NB], int slot) {
  const float t0 = quad_trace(d[0]), t1 = quad_trace(d[1]), t2 = quad_trace(d[2]), t3 = quad_trace(d[3]);
  const float t = (slot & 2) ? ((slot & 1) ? t3 : t2) : ((slot & 1) ? t1 : t0);
  return rbf(rbf(t) * SCALE) * LOG2E;
}
template <int I> __device__ __forceinline__ u32 quad_bc(u32 x) { return dppu<I == 0 ? BC0 : I == 1 ? BC1 : I == 2 ? BC2 : BC3>(x); }

__global__ void __launch_bounds__(NT, 2)
fused_decode(const u32* __restrict__ x32, const char* __restrict__ kc, const char* __restrict__ vc,
             const char* __restrict__ wq, const char* __restrict__ wo, const char* __restrict__ wg,
             const char* __restrict__ wu, const char* __restrict__ wd, u32* __restrict__ out32, char* ws) {
  __shared__ __attribute__((aligned(16))) float lds[5 * NW * 128 + 64 + 2 * NW + 4];
  u32* xn_s = reinterpret_cast<u32*>(lds);
  float* qp_s = lds + NW * 128;
  float* fp_s = qp_s + NW * 128;
  float* op_s = fp_s + NW * 128;
  float* yp_s = op_s + NW * 128;
  u32* q_s = reinterpret_cast<u32*>(yp_s + NW * 128);
  float* ml_s = yp_s + NW * 128 + 64;
  u32* role_s = reinterpret_cast<u32*>(ml_s + 2 * NW);

  asm volatile("" :: "s"(x32), "s"(kc), "s"(vc), "s"(wq), "s"(wo), "s"(wg), "s"(wu), "s"(wd), "s"(out32), "s"(ws));
  const int tid = threadIdx.x, lane = tid & 63, r = lane >> 4, c = lane & 15;
  const int w = __builtin_amdgcn_readfirstlane(tid >> 6);
  const int b = blockIdx.x, xs = b & 7, j = b >> 3;
  const int h = xs * 4 + (j >> 3), s = j & 7;
  const rsrc_t wsr = mkrsrc(ws, WS_END);
  const f32x4 zero4 = {0.f, 0.f, 0.f, 0.f};

  const int qrow = s * QROWS + 4 * w;
  const bool ffn_on = b < FLINES * 8;
  const int frow = xs * 80 + 8 * (w & 3);
  const size_t kv0 = (size_t)(h * NSPLIT + s) * KV_BYTES;
  const rsrc_t kr = mkrsrc(kc + kv0, KV_BYTES), vr = mkrsrc(vc + kv0, KV_BYTES);
  const u32 vo = lane * 16 + w * NB * 1024;
  const u32 ko = (c >> 2) * 256 + (c & 3) * 64 + r * 16 + w * NB * 1024;
  u32 bo = ((u32)(h * NSPLIT + s) / NB) * BATCH - BATCH;
  u32x4 L[2 * NLD];
  auto request = [&](int n) {
    if (n % NB == 0) { bo = (bo + BATCH) & (KV_BYTES - 1); asm volatile("" : "+s"(bo)); }
    L[n] = n < NLD ? bld<NT_>(kr, ko + (n % NB) * 1024, bo) : bld<NT_>(vr, vo + (n % NB) * 1024, bo);
  };
  auto request_range = [&](int n0, int n1) {
#pragma unroll
    for (int n = n0; n < n1; ++n) request(n);
    SB;
  };
  auto request_paced = [&](int n0, int n1) {
#pragma unroll
    for (int n = n0; n < n1; ++n) { PACE(); request(n); SB; }
  };
  const rsrc_t xrs = mkrsrc(x32, DM * 2);
  const u32x4 xa = bld<0>(xrs, lane * 16, 0), xb = bld<0>(xrs, lane * 16 + 1024, 0);
  u32 tag = *(const CAS u32*)(ws + WS_EPOCH) + 1u;
  u32x4 WQ[3];
  {
    const rsrc_t qr = mkrsrc(wq, DM * NH * DH * 2);
    const u32 o = (u32)((qrow + r) * (NH * DH) + h * DH + 8 * c) * 2;
    WQ[0] = bld<NT_>(qr, o, 0);
    WQ[1] = bld<NT_>(qr, o + 32 * NH * DH * 2, 0);
    WQ[2] = bld<NT_>(qr, w < 4 ? o + 64 * NH * DH * 2 : OOB, 0);
    SB;
  }
  request_range(0, G1);

  {
    float xv[16], sum = 0.f;
#pragma unroll
    for (int i = 0; i < 4; ++i) { xv[2 * i] = lo16(xa[i]); xv[2 * i + 1] = hi16(xa[i]); xv[8 + 2 * i] = lo16(xb[i]); xv[9 + 2 * i] = hi16(xb[i]); }
#pragma unroll
    for (int i = 0; i < 16; ++i) sum += xv[i];
    const float mean = xrow_sum(row_sum(sum)) * (1.f / DM);
    float sq = 0.f, sq2 = 0.f;
#pragma unroll
    for (int i = 0; i < 8; ++i) { const float d = xv[i] - mean, e = xv[8 + i] - mean; sq = fmaf(d, d, sq); sq2 = fmaf(e, e, sq2); }
    const float var = xrow_sum(row_sum(lane < 16 ? sq + sq2 : sq)) * (1.f / DM);
    const float mb = rbf(mean);
    const float inv = __builtin_amdgcn_rcpf(rbf(__builtin_amdgcn_sqrtf(rbf(rbf(var) + 1e-5f))));
    u32 n[16];
#pragma unroll
    for (int i = 0; i < 16; ++i) { const float t = rbf(xv[i] - mb) * inv; n[i] = pk_bf16(t, t); }
    *reinterpret_cast<u32x4*>(xn_s + 8 * lane) = u32x4{n[0], n[1], n[2], n[3]};
    *reinterpret_cast<u32x4*>(xn_s + 8 * lane + 4) = u32x4{n[4], n[5], n[6], n[7]};
    *reinterpret_cast<u32x4*>(xn_s + 512 + 8 * lane) = u32x4{n[8], n[9], n[10], n[11]};
    *reinterpret_cast<u32x4*>(xn_s + 512 + 8 * lane + 4) = u32x4{n[12], n[13], n[14], n[15]};
  }
  SB;
  request_range(G1, G2);

  const u32x4 sel16 = diag_sel(c);
  {
    f32x4 a = zero4;
#pragma unroll
    for (int i = 0; i < 3; ++i) a = vecmat(WQ[i], xn_s[qrow + 32 * i + r], sel16, a);
    if (c < 8) *reinterpret_cast<f32x4*>(qp_s + w * 128 + (r * 8 + c) * 4) = a;
  }
  asm volatile("" : "+s"(tag));
  __syncthreads();
  if (w == 0) {
    const float* p = qp_s + acc_pos(2 * lane);
    float t0 = 0.f, t1 = 0.f;
#pragma unroll
    for (int i = 0; i < NW; ++i) { t0 += p[i * 128]; t1 += p[i * 128 + 4]; }
    st_unit(wsr, WS_QP + (h * NSPLIT + s) * 1024 + lane * 16, t0, t1, tag);
    if (b == 0 && lane == 0)
      __hip_atomic_store((GAS u32*)(ws + WS_TICKET), (tag << 12) + TICKET0, __ATOMIC_RELAXED, __HIP_MEMORY_SCOPE_AGENT);
  }
  SB;

  u32x4 WF[3];
  {
    const rsrc_t fr = mkrsrc(w < 4 ? wg : wu, ffn_on ? DM * FF * 2 : 0);
    const u32 o = (u32)((frow + (lane >> 3)) * FF + j * 64 + 8 * (lane & 7)) * 2;
    PACE(); WF[0] = bld<NT_>(fr, o, 0); SB;
    PACE(); WF[1] = bld<NT_>(fr, o + 32 * FF * 2, 0); SB;
    PACE(); WF[2] = bld<NT_>(fr, (w & 3) < 2 ? o + 64 * FF * 2 : OOB, 0);
    SB;
  }
  u32 touch = 0;
  if (w == 5) {
    const char* p = lane == 0 ? vc + kv0 : lane < 4 ? wo + (lane - 1) * 2097152 : wd + (lane & 1) * 1048576;
    if (lane < 6) touch = __builtin_nontemporal_load((const u32*)(p + (lane ? b * 4096 : 0)));
  }
  SB;
  request_paced(G2, G3);
  const u32 qoff = WS_QP + h * NSPLIT * 1024 + lane * 16;
  u32x4 qu[NSPLIT];
  if (w == 0) {
#pragma unroll
    for (int i = 0; i < NSPLIT; ++i) qu[i] = ld_unit(wsr, qoff + i * 1024);
  }
  SB;
  request_paced(G3, NPRE);
  if (w == 0) {
    auto all_fresh = [&]() {
      bool ok = true;
#pragma unroll
      for (int i = 0; i < NSPLIT; ++i) ok = ok && fresh(qu[i], tag);
      return __all(ok);
    };
    if (!all_fresh()) {
#pragma nounroll
      for (u32 spin = 0; spin < SPIN_CAP; ++spin) {
        asm volatile("" ::: "memory");
#pragma unroll
        for (int i = 0; i < NSPLIT; ++i) qu[i] = ld_unit(wsr, qoff + i * 1024);
        if (all_fresh()) break;
      }
    }
    float t0 = 0.f, t1 = 0.f;
#pragma unroll
    for (int i = 0; i < NSPLIT; ++i) { t0 += asf(qu[i][0]); t1 += asf(qu[i][2]); }
    q_s[lane] = pk_bf16(t0, t1);
  }
  {
    const u32x4 sel8 = diag_sel(lane & 7);
    f32x4 f = zero4;
#pragma unroll
    for (int i = 0; i < 3; ++i) f = vecmat(WF[i], xn_s[frow + 32 * i + (lane >> 3)], sel8, f);
    if ((c >> 3) == (r >> 1)) *reinterpret_cast<f32x4*>(fp_s + w * 128 + (r * 8 + (c & 7)) * 4) = f;
  }
  __syncthreads();
  const u32x4 q = *reinterpret_cast<const u32x4*>(q_s + 16 * (c & 3) + 4 * r);
  if (w == NW - 1 && ffn_on) {
    const float* p = fp_s + (lane >> 5) * 512 + acc_pos(2 * (lane & 31));
    float g0 = 0.f, g1 = 0.f;
#pragma unroll
    for (int i = 0; i < 8; ++i) { g0 += p[i * 64]; g1 += p[i * 64 + 4]; }
    st_unit<SC1 | NT_>(wsr, WS_HG + b * 1024 + lane * 16, g0, g1, tag);
  }

  auto refill = [&](int n) {
    const int nx = n < RING ? NPRE + n : n >= NPRE ? n + RING : 2 * NLD;
    if (nx < 2 * NLD) { SB; request(nx); SB; }
  };
  float sv[NBT];
#pragma unroll
  for (int t = 0; t < NBT; ++t) {
    f32x4 d[NB];
#pragma unroll
    for (int i = 0; i < NB; ++i) { d[i] = mm(L[NB * t + i], q, zero4); refill(NB * t + i); }
    sv[t] = batch_score(d, c & 3);
    asm volatile("" : "+v"(sv[t]));
  }
  asm volatile("" :: "v"(touch));
  float mx = sv[0];
#pragma unroll
  for (int t = 1; t < NBT; ++t) mx = fmaxf(mx, sv[t]);
  mx = fmaxf(mx, dpp<XOR1>(mx));
  mx = xrow_max(fmaxf(mx, dpp<XOR2>(mx)));
  float pw[NBT], ls = 0.f;
#pragma unroll
  for (int t = 0; t < NBT; ++t) { pw[t] = __builtin_amdgcn_exp2f(sv[t] - mx); ls += pw[t]; }
  ls += dpp<XOR1>(ls);
  ls = xrow_sum(ls + dpp<XOR2>(ls));
  if (lane == 0) *reinterpret_cast<f32x2*>(ml_s + 2 * w) = f32x2{mx, ls};
  asm volatile("s_waitcnt lgkmcnt(0)\n\ts_barrier" ::: "memory");
  u32 pp[NBT];
  {
    const f32x2 ml = *reinterpret_cast<const f32x2*>(ml_s + 2 * (lane >> 3));
    const float M = oct_max(ml[0]), L = oct_sum(__builtin_amdgcn_exp2f(ml[0] - M) * ml[1]);
    st_unit<SC1 | NT_>(wsr, (w == 0 && lane == 0) ? WS_H1 + (h * NSPLIT + s) * REC1 + 1024 : OOB, M, L, tag);
    const float f = __builtin_amdgcn_exp2f(mx - M);
#pragma unroll
    for (int t = 0; t < NBT; ++t) { const float p = pw[t] * f; pp[t] = pk_bf16(p, p); }
  }
  const u32 toff = (w == 1 && lane == 0) ? WS_TICKET : OOB;
  u32 ticket = 0;
  f32x4 acc = zero4;
#pragma unroll
  for (int t = 0; t < NBT - 1; ++t) {
    if (t == NBT - 2) { SB; ticket = __builtin_amdgcn_raw_ptr_buffer_atomic_add_i32(1, wsr, toff, 0, 0); SB; }
    const int n = NLD + NB * t;
    acc = vecmat(L[n], quad_bc<0>(pp[t]), sel16, acc); refill(n);
    acc = vecmat(L[n + 1], quad_bc<1>(pp[t]), sel16, acc); refill(n + 1);
    acc = vecmat(L[n + 2], quad_bc<2>(pp[t]), sel16, acc); refill(n + 2);
    acc = vecmat(L[n + 3], quad_bc<3>(pp[t]), sel16, acc); refill(n + 3);
  }
  u32 opr = (lane >> 3) * DH + acc_pos(16 * w + 2 * (lane & 7)), opw = w * DH + (r * 8 + c) * 4;
  u32 pub = lane < 8 ? WS_H1 + (h * NSPLIT + s) * REC1 + (8 * w + lane) * 16 : OOB;
  asm volatile("" : "+v"(pub), "+v"(opr), "+v"(opw));
  SB;
  {
    constexpr int n = 2 * NLD - NB;
    acc = vecmat(L[n], quad_bc<0>(pp[NBT - 1]), sel16, acc); acc = vecmat(L[n + 1], quad_bc<1>(pp[NBT - 1]), sel16, acc);
    acc = vecmat(L[n + 2], quad_bc<2>(pp[NBT - 1]), sel16, acc); acc = vecmat(L[n + 3], quad_bc<3>(pp[NBT - 1]), sel16, acc);
  }
  if (c < 8) *reinterpret_cast<f32x4*>(op_s + opw) = acc;
  if (w == 1) {
    auto valid = [&](u32 v) { return (v >> 12) == (tag & 0xfffffu) && (v & 0xfffu) >= TICKET0; };
    u32 tk = __builtin_amdgcn_readfirstlane(ticket);
    if (!valid(tk)) {
#pragma nounroll
      for (u32 spin = 0; spin < SPIN_CAP; ++spin) {
        __builtin_amdgcn_s_sleep(8);
        asm volatile("" ::: "memory");
        if (valid(__builtin_amdgcn_readfirstlane(__builtin_amdgcn_raw_buffer_load_b32(wsr, toff, 0, SC1)))) break;
      }
      tk = __builtin_amdgcn_readfirstlane(__builtin_amdgcn_raw_ptr_buffer_atomic_add_i32(1, wsr, toff, 0, 0));
    }
    if (lane == 0) {
      *role_s = (tk & 0xfffu) - TICKET0;
      if ((tk & 0xfffu) == TICKET0 + NWG - 1)
        __hip_atomic_store((GAS u32*)(ws + WS_EPOCH), tag, __ATOMIC_RELAXED, __HIP_MEMORY_SCOPE_AGENT);
    }
  }
  __syncthreads();
  const u32 role = __builtin_amdgcn_readfirstlane(*role_s);
  st_unit(wsr, pub, oct_sum(op_s[opr]), oct_sum(op_s[opr + 4]), tag);
  if (role < NTILE) {
    const int hp = role / NSL, sp = role % NSL;
    const u32 rec = WS_H1 + (hp * NSPLIT + (c >> 1)) * REC1;
    const u32 oa = rec + (8 * w + 2 * r + (c & 1)) * 16, om = rec + 1024;
    const u32x4 nm = ld_unit(wsr, om);
    u32x4 WO[4], WD[2], na;
    u32x2 HG[2];
    u32 hoff[2];
    {
      const rsrc_t wor = mkrsrc(wo, NH * DH * DM * 2), wdr = mkrsrc(wd, FF * DM * 2);
      const u32 o = (u32)((hp * DH + 16 * w + 4 * r) * DM + sp * 128 + 8 * c) * 2;
#pragma unroll
      for (int i = 0; i < 2; ++i) {
        const int fr = 8 * w + 4 * i + r, f = hp * FROWS + (fr < FROWS ? fr : 0);
        hoff[i] = WS_HG + ((f >> 6) * 8 + (c & 7)) * 1024 + (c >> 3) * 512 + (f & 63) * 8;
        HG[i] = ld_gran(wsr, hoff[i]);
        WD[i] = bld<NT_>(wdr, fr < FROWS ? (u32)(f * DM + sp * 128 + 8 * c) * 2 : OOB, 0);
      }
#pragma unroll
      for (int i = 0; i < 4; ++i) WO[i] = bld<NT_>(wor, o + i * DM * 2, 0);
      SB;
      na = ld_unit(wsr, oa);
      SB;
    }
    if (!__all(HG[0][1] == tag && HG[1][1] == tag)) {
#pragma nounroll
      for (u32 spin = 0; spin < SPIN_CAP; ++spin) {
        asm volatile("" ::: "memory");
#pragma unroll
        for (int i = 0; i < 2; ++i) HG[i] = ld_gran(wsr, hoff[i]);
        if (__all(HG[0][1] == tag && HG[1][1] == tag)) break;
        __builtin_amdgcn_s_sleep(1);
      }
    }
    f32x4 y = zero4;
#pragma unroll
    for (int i = 0; i < 2; ++i) {
      float t = asf(HG[i][0]);
      t += dpp<XOR1>(t); t += dpp<XOR2>(t); t += dpp<HMIR>(t);
      const float o = dpp<ROR8>(t);
      const float g = rbf(c < 8 ? t : o), u = rbf(c < 8 ? o : t);
      const float hv = rbf(g * __builtin_amdgcn_rcpf(1.f + __expf(-g))) * u;
      y = vecmat(WD[i], pk_bf16(hv, hv), sel16, y);
    }
    const u32 yc = 16 * w + 2 * (lane & 7);
    u32 ypw = w * 128 + (r * 8 + c) * 4, ypr = (lane >> 3) * 128 + acc_pos(yc);
    u32 pub2 = lane < 8 ? WS_H2 + (sp * NH + hp) * 1024 + yc * 8 : OOB;
    asm volatile("" : "+v"(pub2), "+v"(ypw), "+v"(ypr));
    u32 m0, m1;
    {
      m0 = nm[0]; m1 = nm[2];
      const u64 need = __ballot(!fresh(nm, tag));
      if (need) {
        asm volatile("" : "+v"(na));
        poll_units<3, 2>(wsr, om, tag, need, m0, m1);
        asm volatile("s_waitcnt vmcnt(0)" ::: "memory");
      }
    }
    float M = asf(m0);
    M = fmaxf(M, dpp<XOR2>(M)); M = fmaxf(M, dpp<ROR4>(M)); M = fmaxf(M, dpp<ROR8>(M));
    float f = __builtin_amdgcn_exp2f(asf(m0) - M), L = f * asf(m1);
    L += dpp<XOR2>(L); L += dpp<ROR4>(L); L += dpp<ROR8>(L);
    f *= __builtin_amdgcn_rcpf(L);
    asm volatile("" : "+v"(f));
    u32 a0, a1;
    {
      a0 = na[0]; a1 = na[2];
      const u64 need = __ballot(!fresh(na, tag));
      if (need) poll_units<3, 2>(wsr, oa, tag, need, a0, a1);
    }
    float o0 = f * asf(a0), o1 = f * asf(a1);
    o0 += dpp<XOR2>(o0); o1 += dpp<XOR2>(o1);
    o0 += dpp<ROR4>(o0); o1 += dpp<ROR4>(o1);
    o0 += dpp<ROR8>(o0); o1 += dpp<ROR8>(o1);
    const u32 e0 = pk_bf16(o0, o0), e1 = pk_bf16(o1, o1), n0 = dppu<XOR1>(e0), n1 = dppu<XOR1>(e1);
    const bool odd = c & 1;
    const u32x4 b0 = diag(odd ? n0 : e0, sel16), b1 = diag(odd ? n1 : e1, sel16);
    const u32x4 b2 = diag(odd ? e0 : n0, sel16), b3 = diag(odd ? e1 : n1, sel16);
    f32x4 y2 = mm(WO[1], b1, zero4);
    y = mm(WO[0], b0, y); y2 = mm(WO[3], b3, y2); y = mm(WO[2], b2, y);
    if (c < 8) *reinterpret_cast<f32x4*>(yp_s + ypw) = y + y2;
    asm volatile("s_waitcnt lgkmcnt(0)\n\ts_barrier" ::: "memory");
    st_unit(wsr, pub2, oct_sum(yp_s[ypr]), oct_sum(yp_s[ypr + 4]), tag);
    asm volatile("s_waitcnt vmcnt(0)" ::: "memory");
  } else if (role < NTILE + NRED) {
    const int col = ((role - NTILE) * NW + w) * 4 + 2 * (lane & 1);
    const u32 off = WS_H2 + ((col >> 7) * NH + (lane >> 1)) * 1024 + (col & 127) * 8;
    const u32 xw = x32[col >> 1];
    u32 u0, u1;
    {
      const u32x4 n = ld_unit(wsr, off);
      u0 = n[0]; u1 = n[2];
      const u64 need = __ballot(!fresh(n, tag));
      asm volatile("" :: "v"(xw));
      if (need) poll_units<2, 8>(wsr, off, tag, need, u0, u1);
    }
    float y0 = asf(u0), y1 = asf(u1);
    y0 += dpp<XOR2>(y0); y1 += dpp<XOR2>(y1);
    y0 += dpp<ROR4>(y0); y1 += dpp<ROR4>(y1);
    y0 = oct_sum(y0); y1 = oct_sum(y1);
    if (lane < 2) __builtin_nontemporal_store(pk_bf16(lo16(xw) + y0, hi16(xw) + y1), out32 + (col >> 1));
    asm volatile("s_waitcnt vmcnt(0)" ::: "memory");
  }
}

}

extern "C" void kernel_launch(void* const* d_in, const int* in_sizes, int n_in, void* d_out, int out_size,
                              void* d_ws, size_t ws_size, hipStream_t stream) {
  (void)in_sizes; (void)n_in; (void)out_size; (void)ws_size;
  fused_decode<<<NWG, NT, 0, stream>>>((const u32*)d_in[0], (const char*)d_in[1], (const char*)d_in[2],
                                       (const char*)d_in[3], (const char*)d_in[4], (const char*)d_in[6],
                                       (const char*)d_in[7], (const char*)d_in[8], (u32*)d_out, (char*)d_ws);
}
```

```cpp
#include <hip/hip_runtime.h>
#include <stdint.h>

namespace {

typedef unsigned u32;
typedef unsigned long long u64;
typedef u32 u32x4 __attribute__((ext_vector_type(4)));
typedef u32 u32x2 __attribute__((ext_vector_type(2)));
typedef float f32x2 __attribute__((ext_vector_type(2)));
typedef float f32x4 __attribute__((ext_vector_type(4)));
typedef __bf16 bf16x2 __attribute__((ext_vector_type(2)));
typedef __bf16 bf16x8 __attribute__((ext_vector_type(8)));
typedef __amdgpu_buffer_rsrc_t rsrc_t;
#define GAS __attribute__((address_space(1)))
#define CAS __attribute__((address_space(4)))
#define SB __builtin_amdgcn_sched_barrier(0)
#define PACE() do { __builtin_amdgcn_s_waitcnt((RING - 1) | 0x0F70); SB; } while (0)

constexpr int DM = 640, NH = 32, SK = 16384, DH = 128, FF = 1728;
constexpr int NSPLIT = 8, NWG = NH * NSPLIT, NW = 8, NT = NW * 64;
constexpr int KEYS_WG = SK / NSPLIT;
constexpr int NB = 4;
constexpr u32 KV_BYTES = KEYS_WG * DH * 2, BATCH = NW * NB * 1024;
constexpr int NBT = KV_BYTES / BATCH;
constexpr int NLD = NB * NBT;
constexpr int RING = 4;
constexpr int G1 = 4, G2 = 8, G3 = 11, NPRE = 16;
constexpr int QROWS = DM / NSPLIT;
constexpr int NSL = 5;
constexpr int FROWS = FF / NH;
constexpr int FLINES = FF / 64;
constexpr u32 NTILE = NH * NSL, NRED = DM / (4 * NW);
constexpr u32 OOB = 0x7ff00000u;
constexpr int NT_ = 2, SC1 = 16;

constexpr u32 WS_EPOCH = 0;
constexpr u32 WS_TICKET = 2048;
constexpr u32 WS_QP = 4096;
constexpr u32 WS_HG = WS_QP + NWG * 1024;
constexpr u32 REC1 = 1280;
constexpr u32 WS_H1 = WS_HG + FLINES * 8 * 1024;
constexpr u32 WS_H2 = WS_H1 + NWG * REC1;
constexpr u32 WS_END = WS_H2 + NSL * NH * 1024;
constexpr u32 TICKET0 = 0x400;
constexpr u32 SPIN_CAP = 1u << 18;

constexpr float LOG2E = 1.4426950408889634f;
constexpr float SCALE = 0.08838834764831845f;

__device__ __forceinline__ float asf(u32 x) { return __builtin_bit_cast(float, x); }
__device__ __forceinline__ u32 asu(float x) { return __builtin_bit_cast(u32, x); }
__device__ __forceinline__ float lo16(u32 x) { return asf(x << 16); }
__device__ __forceinline__ float hi16(u32 x) { return asf(x & 0xffff0000u); }
__device__ __forceinline__ u32 pk_bf16(float a, float b) { bf16x2 v = {(__bf16)a, (__bf16)b}; return __builtin_bit_cast(u32, v); }
__device__ __forceinline__ float rbf(float x) { return (float)(__bf16)x; }

template <int CTRL> __device__ __forceinline__ u32 dppu(u32 x) { return __builtin_amdgcn_mov_dpp(x, CTRL, 0xf, 0xf, true); }
template <int CTRL> __device__ __forceinline__ float dpp(float x) { return asf(dppu<CTRL>(asu(x))); }
constexpr int XOR1 = 0xB1, XOR2 = 0x4E, HMIR = 0x141, ROR4 = 0x124, ROR8 = 0x128;
constexpr int BC0 = 0x00, BC1 = 0x55, BC2 = 0xAA, BC3 = 0xFF;
__device__ __forceinline__ float row_sum(float x) {
  x += dpp<XOR1>(x); x += dpp<XOR2>(x); x += dpp<ROR4>(x); x += dpp<ROR8>(x); return x;
}
__device__ __forceinline__ float fold16(float a, float b) {
  auto s = __builtin_amdgcn_permlane16_swap(asu(a), asu(b), false, false);
  return asf(s[0]) + asf(s[1]);
}
__device__ __forceinline__ float fold32(float x) {
  auto s = __builtin_amdgcn_permlane32_swap(asu(x), asu(x), false, false);
  return asf(s[0]) + asf(s[1]);
}
__device__ __forceinline__ float xrow_sum(float x) { return fold32(fold16(x, x)); }
__device__ __forceinline__ float xrow_max(float x) {
  auto s = __builtin_amdgcn_permlane16_swap(asu(x), asu(x), false, false);
  x = fmaxf(asf(s[0]), asf(s[1]));
  auto t = __builtin_amdgcn_permlane32_swap(asu(x), asu(x), false, false);
  return fmaxf(asf(t[0]), asf(t[1]));
}
__device__ __forceinline__ float oct_sum(float x) { return xrow_sum(x + dpp<ROR8>(x)); }
__device__ __forceinline__ float oct_max(float x) { return xrow_max(fmaxf(x, dpp<ROR8>(x))); }

__device__ __forceinline__ f32x4 mm(u32x4 a, u32x4 b, f32x4 acc) {
  return __builtin_amdgcn_mfma_f32_16x16x32_bf16(__builtin_bit_cast(bf16x8, a), __builtin_bit_cast(bf16x8, b), acc, 0, 0, 0);
}
__device__ __forceinline__ u32x4 diag_sel(int j) {
  const u32 half = (j & 1) ? 0xffff0000u : 0x0000ffffu;
  return u32x4{(j >> 1) == 0 ? half : 0u, (j >> 1) == 1 ? half : 0u, (j >> 1) == 2 ? half : 0u, (j >> 1) == 3 ? half : 0u};
}
__device__ __forceinline__ u32x4 diag(u32 xx, u32x4 sel) { return u32x4{xx & sel[0], xx & sel[1], xx & sel[2], xx & sel[3]}; }
__device__ __forceinline__ f32x4 vecmat(u32x4 w, u32 xx, u32x4 sel, f32x4 acc) { return mm(w, diag(xx, sel), acc); }
__device__ __forceinline__ u32 acc_pos(u32 e) { return ((e >> 5) * 8 + (e & 7)) * 4 + ((e >> 3) & 3); }

__device__ __forceinline__ rsrc_t mkrsrc(const void* p, u32 bytes) {
  return __builtin_amdgcn_make_buffer_rsrc(const_cast<void*>(p), 0, bytes, 0x00020000);
}
template <int AUX> __device__ __forceinline__ u32x4 bld(rsrc_t r, u32 voff, u32 soff) {
  return __builtin_amdgcn_raw_buffer_load_b128(r, voff, soff, AUX);
}
template <int AUX = SC1> __device__ __forceinline__ void st_unit(rsrc_t r, u32 off, float a, float b, u32 tag) {
  __builtin_amdgcn_raw_buffer_store_b128(u32x4{asu(a), tag, asu(b), tag}, r, off, 0, AUX);
}
__device__ __forceinline__ u32x4 ld_unit(rsrc_t r, u32 off) { return bld<SC1>(r, off, 0); }
__device__ __forceinline__ u32x2 ld_gran(rsrc_t r, u32 off) { return __builtin_amdgcn_raw_buffer_load_b64(r, off, 0, SC1); }
__device__ __forceinline__ bool fresh(u32x4 u, u32 tag) { return u[1] == tag && u[3] == tag; }

#define POLL_LD(A) "buffer_load_dwordx4 v[" #A ":" #A "+3], %[o], %[rs], 0 offen sc1\n\t"
#define POLL_PASS(A, YOUNGER, L)                            \
  "s_waitcnt vmcnt(" #YOUNGER ")\n\t"                       \
  "v_cmp_eq_u32 vcc, %[tag], v[" #A "+1]\n\t"               \
  "v_cmp_eq_u32 %[t], %[tag], v[" #A "+3]\n\t"              \
  "s_and_b64 vcc, vcc, %[t]\n\t"                            \
  "s_and_b64 vcc, vcc, %[need]\n\t"                         \
  "v_cndmask_b32 %[u0], %[u0], v[" #A "], vcc\n\t"          \
  "v_cndmask_b32 %[u1], %[u1], v[" #A "+2], vcc\n\t"        \
  "s_andn2_b64 %[need], %[need], vcc\n\t"                   \
  "s_cbranch_scc0 2f\n\t"                                   \
  "s_bcnt1_i32_b64 %[n], %[need]\n\t"                       \
  "s_cmp_le_u32 %[n], 8\n\t"                                \
  "s_cbranch_scc1 " L "f\n\t"                               \
  "s_sleep %[slp]\n"                                        \
  L ":\n\t"                                                 \
  "s_mov_b64 exec, %[need]\n\t"                             \
  POLL_LD(A)                                                \
  "s_mov_b64 exec, -1\n\t"
#define POLL_END                                            \
  "s_sub_u32 %[cnt], %[cnt], 1\n\t"                         \
  "s_cbranch_scc0 1b\n"                                     \
  "2:\n\t"                                                  \
  "s_nop 1"
#define POLL_OPERANDS                                                                                     \
      : [u0] "+v"(u0), [u1] "+v"(u1), [need] "+s"(need), [cnt] "+s"(cnt), [t] "=&s"(t), [n] "=&s"(n)      \
      : [o] "v"(o), [rs] "s"(rs), [tag] "s"(tag), [slp] "i"(SLP)                                          \
      : "vcc", "scc", "memory", "v208", "v209", "v210", "v211", "v212", "v213", "v214", "v215", "v216",   \
        "v217", "v218", "v219"
template <int DEPTH, int SLP>
__device__ __forceinline__ void poll_units(rsrc_t rs, u32 o, u32 tag, u64 need, u32& u0, u32& u1) {
  u64 t; u32 n, cnt = SPIN_CAP;
  if constexpr (DEPTH == 3)
    asm volatile("s_mov_b64 exec, %[need]\n\t" POLL_LD(208) "s_sleep 5\n\t" POLL_LD(212) "s_sleep 5\n\t" POLL_LD(216)
                 "s_mov_b64 exec, -1\n1:\n\t" POLL_PASS(208, 2, "3") POLL_PASS(212, 2, "4") POLL_PASS(216, 2, "5") POLL_END
                 POLL_OPERANDS);
  else
    asm volatile("s_mov_b64 exec, %[need]\n\t" POLL_LD(208) "s_sleep 6\n\t" POLL_LD(212)
                 "s_mov_b64 exec, -1\n1:\n\t" POLL_PASS(208, 1, "3") POLL_PASS(212, 1, "4") POLL_END
                 POLL_OPERANDS);
}

__device__ __forceinline__ float quad_trace(f32x4 d) {
  float t = ((dpp<BC0>(d[0]) + dpp<BC1>(d[1])) + dpp<BC2>(d[2])) + dpp<BC3>(d[3]);
  asm("" : "+v"(t));
  return t;
}
__device__ __forceinline__ float batch_score(const f32x4 (&d)[NB], int slot) {
  const float t0 = quad_trace(d[0]), t1 = quad_trace(d[1]), t2 = quad_trace(d[2]), t3 = quad_trace(d[3]);
  const float t = (slot & 2) ? ((slot & 1) ? t3 : t2) : ((slot & 1) ? t1 : t0);
  return rbf(rbf(t) * SCALE) * LOG2E;
}
template <int I> __device__ __forceinline__ u32 quad_bc(u32 x) { return dppu<I == 0 ? BC0 : I == 1 ? BC1 : I == 2 ? BC2 : BC3>(x); }

__global__ void __launch_bounds__(NT, 2)
fused_decode(const u32* __restrict__ x32, const char* __restrict__ kc, const char* __restrict__ vc,
             const char* __restrict__ wq, const char* __restrict__ wo, const char* __restrict__ wg,
             const char* __restrict__ wu, const char* __restrict__ wd, u32* __restrict__ out32, char* ws) {
  __shared__ __attribute__((aligned(16))) float lds[5 * NW * 128 + 64 + 2 * NW + 4];
  u32* xn_s = reinterpret_cast<u32*>(lds);
  float* qp_s = lds + NW * 128;
  float* fp_s = qp_s + NW * 128;
  float* op_s = fp_s + NW * 128;
  float* yp_s = op_s + NW * 128;
  u32* q_s = reinterpret_cast<u32*>(yp_s + NW * 128);
  float* ml_s = yp_s + NW * 128 + 64;
  u32* role_s = reinterpret_cast<u32*>(ml_s + 2 * NW);

  asm volatile("" :: "s"(x32), "s"(kc), "s"(vc), "s"(wq), "s"(wo), "s"(wg), "s"(wu), "s"(wd), "s"(out32), "s"(ws));
  const int tid = threadIdx.x, lane = tid & 63, r = lane >> 4, c = lane & 15;
  const int w = __builtin_amdgcn_readfirstlane(tid >> 6);
  const int b = blockIdx.x, xs = b & 7, j = b >> 3;
  const int h = xs * 4 + (j >> 3), s = j & 7;
  const rsrc_t wsr = mkrsrc(ws, WS_END);
  const f32x4 zero4 = {0.f, 0.f, 0.f, 0.f};

  const int qrow = s * QROWS + 4 * w;
  const bool ffn_on = b < FLINES * 8;
  const int frow = xs * 80 + 8 * (w & 3);
  const size_t kv0 = (size_t)(h * NSPLIT + s) * KV_BYTES;
  const rsrc_t kr = mkrsrc(kc + kv0, KV_BYTES), vr = mkrsrc(vc + kv0, KV_BYTES);
  const u32 vo = lane * 16 + w * NB * 1024;
  const u32 ko = (c >> 2) * 256 + (c & 3) * 64 + r * 16 + w * NB * 1024;
  u32 bo = ((u32)(h * NSPLIT + s) / NB) * BATCH - BATCH;
  u32x4 L[2 * NLD];
  auto request = [&](int n) {
    if (n % NB == 0) { bo = (bo + BATCH) & (KV_BYTES - 1); asm volatile("" : "+s"(bo)); }
    L[n] = n < NLD ? bld<NT_>(kr, ko + (n % NB) * 1024, bo) : bld<NT_>(vr, vo + (n % NB) * 1024, bo);
  };
  auto request_range = [&](int n0, int n1) {
#pragma unroll
    for (int n = n0; n < n1; ++n) request(n);
    SB;
  };
  auto request_paced = [&](int n0, int n1) {
#pragma unroll
    for (int n = n0; n < n1; ++n) { PACE(); request(n); SB; }
  };
  const rsrc_t xrs = mkrsrc(x32, DM * 2);
  const u32x4 xa = bld<0>(xrs, lane * 16, 0), xb = bld<0>(xrs, lane * 16 + 1024, 0);
  u32 tag = *(const CAS u32*)(ws + WS_EPOCH) + 1u;
  u32x4 WQ[3];
  {
    const rsrc_t qr = mkrsrc(wq, DM * NH * DH * 2);
    const u32 o = (u32)((qrow + r) * (NH * DH) + h * DH + 8 * c) * 2;
    WQ[0] = bld<NT_>(qr, o, 0);
    WQ[1] = bld<NT_>(qr, o + 32 * NH * DH * 2, 0);
    WQ[2] = bld<NT_>(qr, w < 4 ? o + 64 * NH * DH * 2 : OOB, 0);
    SB;
  }
  request_range(0, G1);

  {
    float xv[16], sum = 0.f;
#pragma unroll
    for (int i = 0; i < 4; ++i) { xv[2 * i] = lo16(xa[i]); xv[2 * i + 1] = hi16(xa[i]); xv[8 + 2 * i] = lo16(xb[i]); xv[9 + 2 * i] = hi16(xb[i]); }
#pragma unroll
    for (int i = 0; i < 16; ++i) sum += xv[i];
    const float mean = xrow_sum(row_sum(sum)) * (1.f / DM);
    float sq = 0.f, sq2 = 0.f;
#pragma unroll
    for (int i = 0; i < 8; ++i) { const float d = xv[i] - mean, e = xv[8 + i] - mean; sq = fmaf(d, d, sq); sq2 = fmaf(e, e, sq2); }
    const float var = xrow_sum(row_sum(lane < 16 ? sq + sq2 : sq)) * (1.f / DM);
    const float mb = rbf(mean);
    const float inv = __builtin_amdgcn_rcpf(rbf(__builtin_amdgcn_sqrtf(rbf(rbf(var) + 1e-5f))));
    u32 n[16];
#pragma unroll
    for (int i = 0; i < 16; ++i) { const float t = rbf(xv[i] - mb) * inv; n[i] = pk_bf16(t, t); }
    *reinterpret_cast<u32x4*>(xn_s + 8 * lane) = u32x4{n[0], n[1], n[2], n[3]};
    *reinterpret_cast<u32x4*>(xn_s + 8 * lane + 4) = u32x4{n[4], n[5], n[6], n[7]};
    *reinterpret_cast<u32x4*>(xn_s + 512 + 8 * lane) = u32x4{n[8], n[9], n[10], n[11]};
    *reinterpret_cast<u32x4*>(xn_s + 512 + 8 * lane + 4) = u32x4{n[12], n[13], n[14], n[15]};
  }
  SB;
  request_range(G1, G2);

  const u32x4 sel16 = diag_sel(c);
  {
    f32x4 a = zero4;
#pragma unroll
    for (int i = 0; i < 3; ++i) a = vecmat(WQ[i], xn_s[qrow + 32 * i + r], sel16, a);
    if (c < 8) *reinterpret_cast<f32x4*>(qp_s + w * 128 + (r * 8 + c) * 4) = a;
  }
  asm volatile("" : "+s"(tag));
  __syncthreads();
  if (w == 0) {
    const float* p = qp_s + acc_pos(2 * lane);
    float t0 = 0.f, t1 = 0.f;
#pragma unroll
    for (int i = 0; i < NW; ++i) { t0 += p[i * 128]; t1 += p[i * 128 + 4]; }
    st_unit(wsr, WS_QP + (h * NSPLIT + s) * 1024 + lane * 16, t0, t1, tag);
    if (b == 0 && lane == 0)
      __hip_atomic_store((GAS u32*)(ws + WS_TICKET), (tag << 12) + TICKET0, __ATOMIC_RELAXED, __HIP_MEMORY_SCOPE_AGENT);
  }
  SB;

  u32x4 WF[3];
  {
    const rsrc_t fr = mkrsrc(w < 4 ? wg : wu, ffn_on ? DM * FF * 2 : 0);
    const u32 o = (u32)((frow + (lane >> 3)) * FF + j * 64 + 8 * (lane & 7)) * 2;
    PACE(); WF[0] = bld<NT_>(fr, o, 0); SB;
    PACE(); WF[1] = bld<NT_>(fr, o + 32 * FF * 2, 0); SB;
    PACE(); WF[2] = bld<NT_>(fr, (w & 3) < 2 ? o + 64 * FF * 2 : OOB, 0);
    SB;
  }
  request_paced(G2, G3);
  const u32 qoff = WS_QP + h * NSPLIT * 1024 + lane * 16;
  u32x4 qu[NSPLIT];
  if (w == 0) {
#pragma unroll
    for (int i = 0; i < NSPLIT; ++i) qu[i] = ld_unit(wsr, qoff + i * 1024);
  }
  SB;
  request_paced(G3, NPRE);
  if (w == 0) {
    auto all_fresh = [&]() {
      bool ok = true;
#pragma unroll
      for (int i = 0; i < NSPLIT; ++i) ok = ok && fresh(qu[i], tag);
      return __all(ok);
    };
    if (!all_fresh()) {
#pragma nounroll
      for (u32 spin = 0; spin < SPIN_CAP; ++spin) {
        asm volatile("" ::: "memory");
#pragma unroll
        for (int i = 0; i < NSPLIT; ++i) qu[i] = ld_unit(wsr, qoff + i * 1024);
        if (all_fresh()) break;
      }
    }
    float t0 = 0.f, t1 = 0.f;
#pragma unroll
    for (int i = 0; i < NSPLIT; ++i) { t0 += asf(qu[i][0]); t1 += asf(qu[i][2]); }
    q_s[lane] = pk_bf16(t0, t1);
  }
  {
    const u32x4 sel8 = diag_sel(lane & 7);
    f32x4 f = zero4;
#pragma unroll
    for (int i = 0; i < 3; ++i) f = vecmat(WF[i], xn_s[frow + 32 * i + (lane >> 3)], sel8, f);
    if ((c >> 3) == (r >> 1)) *reinterpret_cast<f32x4*>(fp_s + w * 128 + (r * 8 + (c & 7)) * 4) = f;
  }
  __syncthreads();
  const u32x4 q = *reinterpret_cast<const u32x4*>(q_s + 16 * (c & 3) + 4 * r);
  if (w == NW - 1 && ffn_on) {
    const float* p = fp_s + (lane >> 5) * 512 + acc_pos(2 * (lane & 31));
    float g0 = 0.f, g1 = 0.f;
#pragma unroll
    for (int i = 0; i < 8; ++i) { g0 += p[i * 64]; g1 += p[i * 64 + 4]; }
    st_unit<SC1 | NT_>(wsr, WS_HG + b * 1024 + lane * 16, g0, g1, tag);
  }

  auto refill = [&](int n) {
    const int nx = n < RING ? NPRE + n : n >= NPRE ? n + RING : 2 * NLD;
    if (nx < 2 * NLD) { SB; request(nx); SB; }
  };
  float sv[NBT];
#pragma unroll
  for (int t = 0; t < NBT; ++t) {
    f32x4 d[NB];
#pragma unroll
    for (int i = 0; i < NB; ++i) { d[i] = mm(L[NB * t + i], q, zero4); refill(NB * t + i); }
    sv[t] = batch_score(d, c & 3);
    asm volatile("" : "+v"(sv[t]));
  }
  float mx = sv[0];
#pragma unroll
  for (int t = 1; t < NBT; ++t) mx = fmaxf(mx, sv[t]);
  mx = fmaxf(mx, dpp<XOR1>(mx));
  mx = xrow_max(fmaxf(mx, dpp<XOR2>(mx)));
  float pw[NBT], ls = 0.f;
#pragma unroll
  for (int t = 0; t < NBT; ++t) { pw[t] = __builtin_amdgcn_exp2f(sv[t] - mx); ls += pw[t]; }
  ls += dpp<XOR1>(ls);
  ls = xrow_sum(ls + dpp<XOR2>(ls));
  if (lane == 0) *reinterpret_cast<f32x2*>(ml_s + 2 * w) = f32x2{mx, ls};
  asm volatile("s_waitcnt lgkmcnt(0)\n\ts_barrier" ::: "memory");
  u32 pp[NBT];
  {
    const f32x2 ml = *reinterpret_cast<const f32x2*>(ml_s + 2 * (lane >> 3));
    const float M = oct_max(ml[0]), L = oct_sum(__builtin_amdgcn_exp2f(ml[0] - M) * ml[1]);
    st_unit<SC1 | NT_>(wsr, (w == 0 && lane == 0) ? WS_H1 + (h * NSPLIT + s) * REC1 + 1024 : OOB, M, L, tag);
    const float f = __builtin_amdgcn_exp2f(mx - M);
#pragma unroll
    for (int t = 0; t < NBT; ++t) { const float p = pw[t] * f; pp[t] = pk_bf16(p, p); }
  }
  const u32 toff = (w == 1 && lane == 0) ? WS_TICKET : OOB;
  u32 ticket = 0;
  f32x4 acc = zero4;
#pragma unroll
  for (int t = 0; t < NBT - 1; ++t) {
    if (t == NBT - 2) { SB; ticket = __builtin_amdgcn_raw_ptr_buffer_atomic_add_i32(1, wsr, toff, 0, 0); SB; }
    const int n = NLD + NB * t;
    acc = vecmat(L[n], quad_bc<0>(pp[t]), sel16, acc); refill(n);
    acc = vecmat(L[n + 1], quad_bc<1>(pp[t]), sel16, acc); refill(n + 1);
    acc = vecmat(L[n + 2], quad_bc<2>(pp[t]), sel16, acc); refill(n + 2);
    acc = vecmat(L[n + 3], quad_bc<3>(pp[t]), sel16, acc); refill(n + 3);
  }
  u32 opr = (lane >> 3) * DH + acc_pos(16 * w + 2 * (lane & 7)), opw = w * DH + (r * 8 + c) * 4;
  u32 pub = lane < 8 ? WS_H1 + (h * NSPLIT + s) * REC1 + (8 * w + lane) * 16 : OOB;
  asm volatile("" : "+v"(pub), "+v"(opr), "+v"(opw));
  SB;
  {
    constexpr int n = 2 * NLD - NB;
    acc = vecmat(L[n], quad_bc<0>(pp[NBT - 1]), sel16, acc); acc = vecmat(L[n + 1], quad_bc<1>(pp[NBT - 1]), sel16, acc);
    acc = vecmat(L[n + 2], quad_bc<2>(pp[NBT - 1]), sel16, acc); acc = vecmat(L[n + 3], quad_bc<3>(pp[NBT - 1]), sel16, acc);
  }
  if (c < 8) *reinterpret_cast<f32x4*>(op_s + opw) = acc;
  if (w == 1) {
    auto valid = [&](u32 v) { return (v >> 12) == (tag & 0xfffffu) && (v & 0xfffu) >= TICKET0; };
    u32 tk = __builtin_amdgcn_readfirstlane(ticket);
    if (!valid(tk)) {
#pragma nounroll
      for (u32 spin = 0; spin < SPIN_CAP; ++spin) {
        __builtin_amdgcn_s_sleep(8);
        asm volatile("" ::: "memory");
        if (valid(__builtin_amdgcn_readfirstlane(__builtin_amdgcn_raw_buffer_load_b32(wsr, toff, 0, SC1)))) break;
      }
      tk = __builtin_amdgcn_readfirstlane(__builtin_amdgcn_raw_ptr_buffer_atomic_add_i32(1, wsr, toff, 0, 0));
    }
    if (lane == 0) {
      *role_s = (tk & 0xfffu) - TICKET0;
      if ((tk & 0xfffu) == TICKET0 + NWG - 1)
        __hip_atomic_store((GAS u32*)(ws + WS_EPOCH), tag, __ATOMIC_RELAXED, __HIP_MEMORY_SCOPE_AGENT);
    }
  }
  __syncthreads();
  const u32 role = __builtin_amdgcn_readfirstlane(*role_s);
  st_unit(wsr, pub, oct_sum(op_s[opr]), oct_sum(op_s[opr + 4]), tag);
  if (role < NTILE) {
    const int hp = role / NSL, sp = role % NSL;
    const u32 rec = WS_H1 + (hp * NSPLIT + (c >> 1)) * REC1;
    const u32 oa = rec + (8 * w + 2 * r + (c & 1)) * 16, om = rec + 1024;
    const u32x4 nm = ld_unit(wsr, om);
    u32x4 WO[4], WD[2], na;
    u32x2 HG[2];
    u32 hoff[2];
    {
      const rsrc_t wor = mkrsrc(wo, NH * DH * DM * 2), wdr = mkrsrc(wd, FF * DM * 2);
      const u32 o = (u32)((hp * DH + 16 * w + 4 * r) * DM + sp * 128 + 8 * c) * 2;
#pragma unroll
      for (int i = 0; i < 2; ++i) {
        const int fr = 8 * w + 4 * i + r, f = hp * FROWS + (fr < FROWS ? fr : 0);
        hoff[i] = WS_HG + ((f >> 6) * 8 + (c & 7)) * 1024 + (c >> 3) * 512 + (f & 63) * 8;
        HG[i] = ld_gran(wsr, hoff[i]);
        WD[i] = bld<NT_>(wdr, fr < FROWS ? (u32)(f * DM + sp * 128 + 8 * c) * 2 : OOB, 0);
      }
#pragma unroll
      for (int i = 0; i < 4; ++i) WO[i] = bld<NT_>(wor, o + i * DM * 2, 0);
      SB;
      na = ld_unit(wsr, oa);
      SB;
    }
    if (!__all(HG[0][1] == tag && HG[1][1] == tag)) {
#pragma nounroll
      for (u32 spin = 0; spin < SPIN_CAP; ++spin) {
        asm volatile("" ::: "memory");
#pragma unroll
        for (int i = 0; i < 2; ++i) HG[i] = ld_gran(wsr, hoff[i]);
        if (__all(HG[0][1] == tag && HG[1][1] == tag)) break;
        __builtin_amdgcn_s_sleep(1);
      }
    }
    f32x4 y = zero4;
#pragma unroll
    for (int i = 0; i < 2; ++i) {
      float t = asf(HG[i][0]);
      t += dpp<XOR1>(t); t += dpp<XOR2>(t); t += dpp<HMIR>(t);
      const float o = dpp<ROR8>(t);
      const float g = rbf(c < 8 ? t : o), u = rbf(c < 8 ? o : t);
      const float hv = rbf(g * __builtin_amdgcn_rcpf(1.f + __expf(-g))) * u;
      y = vecmat(WD[i], pk_bf16(hv, hv), sel16, y);
    }
    const u32 yc = 16 * w + 2 * (lane & 7);
    u32 ypw = w * 128 + (r * 8 + c) * 4, ypr = (lane >> 3) * 128 + acc_pos(yc);
    u32 pub2 = lane < 8 ? WS_H2 + (sp * NH + hp) * 1024 + yc * 8 : OOB;
    asm volatile("" : "+v"(pub2), "+v"(ypw), "+v"(ypr));
    u32 m0, m1;
    {
      m0 = nm[0]; m1 = nm[2];
      const u64 need = __ballot(!fresh(nm, tag));
      if (need) {
        asm volatile("" : "+v"(na));
        poll_units<3, 2>(wsr, om, tag, need, m0, m1);
        asm volatile("s_waitcnt vmcnt(0)" ::: "memory");
      }
    }
    float M = asf(m0);
    M = fmaxf(M, dpp<XOR2>(M)); M = fmaxf(M, dpp<ROR4>(M)); M = fmaxf(M, dpp<ROR8>(M));
    float f = __builtin_amdgcn_exp2f(asf(m0) - M), L = f * asf(m1);
    L += dpp<XOR2>(L); L += dpp<ROR4>(L); L += dpp<ROR8>(L);
    f *= __builtin_amdgcn_rcpf(L);
    asm volatile("" : "+v"(f));
    u32 a0, a1;
    {
      a0 = na[0]; a1 = na[2];
      const u64 need = __ballot(!fresh(na, tag));
      if (need) poll_units<3, 2>(wsr, oa, tag, need, a0, a1);
    }
    float o0 = f * asf(a0), o1 = f * asf(a1);
    o0 += dpp<XOR2>(o0); o1 += dpp<XOR2>(o1);
    o0 += dpp<ROR4>(o0); o1 += dpp<ROR4>(o1);
    o0 += dpp<ROR8>(o0); o1 += dpp<ROR8>(o1);
    const u32 e0 = pk_bf16(o0, o0), e1 = pk_bf16(o1, o1), n0 = dppu<XOR1>(e0), n1 = dppu<XOR1>(e1);
    const bool odd = c & 1;
    const u32x4 b0 = diag(odd ? n0 : e0, sel16), b1 = diag(odd ? n1 : e1, sel16);
    const u32x4 b2 = diag(odd ? e0 : n0, sel16), b3 = diag(odd ? e1 : n1, sel16);
    f32x4 y2 = mm(WO[1], b1, zero4);
    y = mm(WO[0], b0, y); y2 = mm(WO[3], b3, y2); y = mm(WO[2], b2, y);
    if (c < 8) *reinterpret_cast<f32x4*>(yp_s + ypw) = y + y2;
    asm volatile("s_waitcnt lgkmcnt(0)\n\ts_barrier" ::: "memory");
    st_unit(wsr, pub2, oct_sum(yp_s[ypr]), oct_sum(yp_s[ypr + 4]), tag);
    asm volatile("s_waitcnt vmcnt(0)" ::: "memory");
  } else if (role < NTILE + NRED) {
    const int col = ((role - NTILE) * NW + w) * 4 + 2 * (lane & 1);
    const u32 off = WS_H2 + ((col >> 7) * NH + (lane >> 1)) * 1024 + (col & 127) * 8;
    const u32 xw = x32[col >> 1];
    u32 u0, u1;
    {
      const u32x4 n = ld_unit(wsr, off);
      u0 = n[0]; u1 = n[2];
      const u64 need = __ballot(!fresh(n, tag));
      asm volatile("" :: "v"(xw));
      if (need) poll_units<2, 8>(wsr, off, tag, need, u0, u1);
    }
    float y0 = asf(u0), y1 = asf(u1);
    y0 += dpp<XOR2>(y0); y1 += dpp<XOR2>(y1);
    y0 += dpp<ROR4>(y0); y1 += dpp<ROR4>(y1);
    y0 = oct_sum(y0); y1 = oct_sum(y1);
    if (lane < 2) __builtin_nontemporal_store(pk_bf16(lo16(xw) + y0, hi16(xw) + y1), out32 + (col >> 1));
    asm volatile("s_waitcnt vmcnt(0)" ::: "memory");
  }
}

}

extern "C" void kernel_launch(void* const* d_in, const int* in_sizes, int n_in, void* d_out, int out_size,
                              void* d_ws, size_t ws_size, hipStream_t stream) {
  (void)in_sizes; (void)n_in; (void)out_size; (void)ws_size;
  fused_decode<<<NWG, NT, 0, stream>>>((const u32*)d_in[0], (const char*)d_in[1], (const char*)d_in[2],
                                       (const char*)d_in[3], (const char*)d_in[4], (const char*)d_in[6],
                                       (const char*)d_in[7], (const char*)d_in[8], (u32*)d_out, (char*)d_ws);
}
```
